```python
import torch

def _softmax(x, axis=-1):
    return torch.softmax(x, dim=axis)
def _gelu(x):
    return torch.nn.functional.gelu(x, approximate="tanh")
def _silu(x):
    return torch.nn.functional.silu(x)
def _sigmoid(x):
    return torch.sigmoid(x)
def _layernorm(x, axis=-1, eps=1e-5):
    m = x.mean(dim=axis, keepdim=True)
    v = x.var(dim=axis, keepdim=True, unbiased=False)
    return (x - m) / torch.sqrt(v + eps)
def _scatter_add(tgt, idx, src, axis=0):
    return tgt.clone().index_add_(axis, idx, src)
def _segment_sum(src, seg, n):
    out = torch.zeros((n,) + src.shape[1:], dtype=src.dtype)
    return out.index_add_(0, seg, src)
def _pad(x, pw):
    flat = tuple(v for pair in reversed(pw) for v in pair)
    return torch.nn.functional.pad(x, flat)

kernel_name = 'concat_heads__grouped_kv_mqa__spectral_fnet__prompt'

def setup_inputs():

    _w = (lambda a: (lambda t: t.to(torch.bfloat16) if t.is_floating_point() else t)(torch.as_tensor(a)))
    def _A_setup():
        B, S, D, H = 1, 2048, 1024, 32
        Dh = D // H
        return {
            "x": _w((lambda *s: torch.randn(*s, dtype=torch.bfloat16) * 0.05)(B, S, D)),
            "wq": _w((lambda *s: torch.randn(*s, dtype=torch.bfloat16) * 3.2)(D, H * Dh)),
            "wk": _w((lambda *s: torch.randn(*s, dtype=torch.bfloat16) * 3.2)(D, Dh)),
            "wv": _w((lambda *s: torch.randn(*s, dtype=torch.bfloat16) * 0.4)(D, Dh)),
            "wo": _w((lambda *s: torch.randn(*s, dtype=torch.bfloat16) * 0.4)(H * Dh, D)),
        }
    _A_in = _A_setup()

    def _B_setup():
        B, S, D = 1, 2048, 1024
        ns = torch.arange(S)[:, None] * torch.arange(S)[None, :]
        nd = torch.arange(D)[:, None] * torch.arange(D)[None, :]
        Ws_re = torch.cos(-2.0 * torch.pi * ns / S).to(float)
        Ws_im = torch.sin(-2.0 * torch.pi * ns / S).to(float)
        Wd_re = torch.cos(-2.0 * torch.pi * nd / D).to(float)
        Wd_im = torch.sin(-2.0 * torch.pi * nd / D).to(float)
        return {
            "x": _w((lambda *s: torch.randn(*s, dtype=torch.bfloat16) * 0.05)(B, S, D)),
            "dft_seq_re": _w(Ws_re),
            "dft_seq_im": _w(Ws_im),
            "dft_hid_re": _w(Wd_re),
            "dft_hid_im": _w(Wd_im),
        }
    _B_in = _B_setup()

    return {
        "A_x": _A_in["x"],
        "A_wq": _A_in["wq"],
        "A_wk": _A_in["wk"],
        "A_wv": _A_in["wv"],
        "A_wo": _A_in["wo"],
        "B_x": _B_in["x"],
        "B_dft_seq_re": _B_in["dft_seq_re"],
        "B_dft_seq_im": _B_in["dft_seq_im"],
        "B_dft_hid_re": _B_in["dft_hid_re"],
        "B_dft_hid_im": _B_in["dft_hid_im"],
    }

def reference(A_x, A_wq, A_wk, A_wv, A_wo, B_x, B_dft_seq_re, B_dft_seq_im, B_dft_hid_re, B_dft_hid_im):
    def _A_ref(x, wq, wk, wv, wo):
        B, S, D = x.shape
        H = 32
        Dh = D // H
        q = (lambda x, *d: x.permute(*d))((x @ wq).reshape(B, S, H, Dh), 0, 2, 1, 3)
        k = x @ wk
        v = x @ wv
        scores = torch.einsum("bhqd,bkd->bhqk", q, k) * (Dh ** -0.5)
        p = _softmax(scores, axis=-1)
        o = torch.einsum("bhqk,bkd->bhqd", p, v)
        return (lambda x, *d: x.permute(*d))(o, 0, 2, 1, 3).reshape(B, S, H * Dh) @ wo

    def _B_ref(x, dft_seq_re, dft_seq_im, dft_hid_re, dft_hid_im):
        a = torch.einsum("bsd,ed->bse", x, dft_hid_re)
        b = torch.einsum("bsd,ed->bse", x, dft_hid_im)
        y = torch.einsum("ts,bse->bte", dft_seq_re, a) - torch.einsum("ts,bse->bte", dft_seq_im, b)
        return y

    a = _A_ref(A_x, A_wq, A_wk, A_wv, A_wo)
    b = _B_ref(B_x, B_dft_seq_re, B_dft_seq_im, B_dft_hid_re, B_dft_hid_im)
    return torch.concatenate([a, b], axis=-1)
```

```cpp
#include <hip/hip_runtime.h>
#include <hip/hip_bf16.h>
typedef unsigned short u16;
__device__ __forceinline__ float bf2f(u16 v) { return __uint_as_float(((unsigned)v) << 16); }
__device__ __forceinline__ u16 f2bf(float f) { __hip_bfloat16 b = __float2bfloat16(f); return *reinterpret_cast<u16*>(&b); }

template <bool TB>
__global__ void gemm_naive(const u16* A, int lda, const u16* B, int ldb, const u16* A2, const u16* B2, u16* C, int ldc, int M, int N, int K, float scale) {
    __shared__ float As[16][17], Bs[16][17];
    int tx = threadIdx.x, ty = threadIdx.y;
    int m = blockIdx.y * 16 + ty, n = blockIdx.x * 16 + tx;
    float acc = 0.f;
    for (int pass = 0; pass < 2; ++pass) {
        const u16* a = pass ? A2 : A; const u16* b = pass ? B2 : B;
        if (!a) break;
        float sgn = pass ? -1.f : 1.f;
        for (int k0 = 0; k0 < K; k0 += 16) {
            As[ty][tx] = bf2f(a[(size_t)m * lda + k0 + tx]);
            if (TB) Bs[tx][ty] = bf2f(b[(size_t)(blockIdx.x * 16 + ty) * ldb + k0 + tx]);
            else Bs[ty][tx] = bf2f(b[(size_t)(k0 + ty) * ldb + n]);
            __syncthreads();
            for (int k = 0; k < 16; ++k) acc += sgn * As[ty][k] * Bs[k][tx];
            __syncthreads();
        }
    }
    C[(size_t)m * ldc + n] = f2bf(acc * scale);
}

__global__ void attn_naive(const u16* Q, const u16* Kp, const u16* V, u16* O) {
    __shared__ float Ks[64][32], Vs[64][32];
    int h = blockIdx.y, pos = blockIdx.x * 256 + threadIdx.x;
    float q[32], o[32];
    for (int d = 0; d < 32; ++d) { q[d] = bf2f(Q[(size_t)pos * 1024 + h * 32 + d]); o[d] = 0.f; }
    float m = -INFINITY, l = 0.f;
    for (int k0 = 0; k0 < 2048; k0 += 64) {
        __syncthreads();
        for (int i = threadIdx.x; i < 64 * 32; i += 256) { Ks[i >> 5][i & 31] = bf2f(Kp[(size_t)k0 * 32 + i]); Vs[i >> 5][i & 31] = bf2f(V[(size_t)k0 * 32 + i]); }
        __syncthreads();
        for (int j = 0; j < 64; ++j) {
            float s = 0.f;
            for (int d = 0; d < 32; ++d) s += q[d] * Ks[j][d];
            if (s > m) { float a = exp2f(m - s); l *= a; for (int d = 0; d < 32; ++d) o[d] *= a; m = s; }
            float p = bf2f(f2bf(exp2f(s - m)));
            l += p;
            for (int d = 0; d < 32; ++d) o[d] += p * Vs[j][d];
        }
    }
    for (int d = 0; d < 32; ++d) O[(size_t)pos * 1024 + h * 32 + d] = f2bf(o[d] / l);
}

extern "C" void kernel_launch(void* const* d_in, const int* in_sizes, int n_in, void* d_out, int out_size, void* d_ws, size_t ws_size, hipStream_t stream) {
    const u16* xA = (const u16*)d_in[0]; const u16* wq = (const u16*)d_in[1]; const u16* wk = (const u16*)d_in[2]; const u16* wv = (const u16*)d_in[3]; const u16* wo = (const u16*)d_in[4];
    const u16* xB = (const u16*)d_in[5]; const u16* sre = (const u16*)d_in[6]; const u16* sim = (const u16*)d_in[7]; const u16* dre = (const u16*)d_in[8]; const u16* dim = (const u16*)d_in[9];
    u16* out = (u16*)d_out;
    u16* ws = (u16*)d_ws;
    u16* Q = ws; u16* Kp = Q + 2048 * 1024; u16* V = Kp + 2048 * 32; u16* O = V + 2048 * 32; u16* a = O + 2048 * 1024; u16* b = a + 2048 * 1024;
    dim3 blk(16, 16);
    const float c = 0.17677669529663687f * 1.4426950408889634f;
    gemm_naive<false><<<dim3(64, 128), blk, 0, stream>>>(xA, 1024, wq, 1024, nullptr, nullptr, Q, 1024, 2048, 1024, 1024, c);
    gemm_naive<false><<<dim3(2, 128), blk, 0, stream>>>(xA, 1024, wk, 32, nullptr, nullptr, Kp, 32, 2048, 32, 1024, 1.f);
    gemm_naive<false><<<dim3(2, 128), blk, 0, stream>>>(xA, 1024, wv, 32, nullptr, nullptr, V, 32, 2048, 32, 1024, 1.f);
    gemm_naive<true><<<dim3(64, 128), blk, 0, stream>>>(xB, 1024, dre, 1024, nullptr, nullptr, a, 1024, 2048, 1024, 1024, 1.f);
    gemm_naive<true><<<dim3(64, 128), blk, 0, stream>>>(xB, 1024, dim, 1024, nullptr, nullptr, b, 1024, 2048, 1024, 1024, 1.f);
    attn_naive<<<dim3(8, 32), 256, 0, stream>>>(Q, Kp, V, O);
    gemm_naive<false><<<dim3(64, 128), blk, 0, stream>>>(O, 1024, wo, 1024, nullptr, nullptr, out, 2048, 2048, 1024, 1024, 1.f);
    gemm_naive<false><<<dim3(64, 128), blk, 0, stream>>>(sre, 2048, a, 1024, sim, b, out + 1024, 2048, 2048, 1024, 2048, 1.f);
}
```

```cpp
#include <hip/hip_runtime.h>
#include <hip/hip_bf16.h>
#include <type_traits>
typedef unsigned short u16;
typedef short bf16x8 __attribute__((ext_vector_type(8)));
typedef short s16x4 __attribute__((ext_vector_type(4)));
typedef float f32x16 __attribute__((ext_vector_type(16)));
typedef float f32x4 __attribute__((ext_vector_type(4)));
typedef float f32x2 __attribute__((ext_vector_type(2)));
typedef unsigned u32x4 __attribute__((ext_vector_type(4)));
typedef __bf16 bf16x2_t __attribute__((ext_vector_type(2)));
#define LAS __attribute__((address_space(3)))
typedef LAS const char* lds_cptr;
#define MFMA32(a, b, c) __builtin_amdgcn_mfma_f32_32x32x16_bf16(a, b, c, 0, 0, 0)
#define MFMA16(a, b, c) __builtin_amdgcn_mfma_f32_16x16x32_bf16(a, b, c, 0, 0, 0)
#define SBAR() __builtin_amdgcn_sched_barrier(0)
#define PIN(x) asm volatile("" : "+v"(x))
#define DI __device__ __forceinline__
DI unsigned cvtpk(float lo, float hi) { f32x2 v = {lo, hi}; bf16x2_t r = __builtin_convertvector(v, bf16x2_t); return __builtin_bit_cast(unsigned, r); }
template <int OFF> DI void glds16(unsigned voff, const void* sbase) {
    asm volatile("global_load_lds_dwordx4 %0, %1 offset:%c2" ::"v"(voff), "s"(sbase), "i"(OFF) : "memory");
}
DI void set_m0(unsigned v) { asm volatile("s_mov_b32 m0, %0\n\ts_nop 0" ::"s"(v) : "memory"); }
template <int N> DI void wait_vm() { asm volatile("s_waitcnt vmcnt(%c0)" ::"i"(N) : "memory"); }
DI void lds_bar() { __builtin_amdgcn_s_waitcnt(0xC07F); asm volatile("" ::: "memory"); __builtin_amdgcn_s_barrier(); asm volatile("" ::: "memory"); }
DI int pi32(int r) { return ((r >> 2) & 1) * 16 + ((r >> 4) & 1) * 8 + ((r >> 3) & 1) * 4 + (r & 3); }

template <int BM, int BN, int WTM, int WTN, int KG, int S, bool NN, bool SWAP = false>
struct Gemm3 {
    static constexpr int NWM = BM / WTM, NWN = BN / WTN, NW = NWM * NWN * KG;
    static_assert(NW == 4, "4 waves");
    static constexpr int XB = BM * 128, WB = BN * 128, SB = XB + WB;
    static constexpr int NIX = (BM / 8) / NW, NIW = (BN / 8) / NW, NI = NIX + NIW;
    static constexpr int TM = WTM / 32, TN = WTN / 32, KS = 4 / KG;
    static constexpr int LDS_BYTES = S * SB;
    static_assert(NIX <= 8 && NIW <= 4 && NIW >= 1 && (S - 1) * NI <= 63, "");
    struct Frag { bf16x8 x[TM]; bf16x8 w[TN]; };

    static DI void run(f32x16 (&acc)[TM][TN], const u16* Xp, int ldx, const u16* Wp, int ldw, const u16* X2p, const u16* W2p, int seg, int nkt, char* lds_generic) {
        const int tid = threadIdx.x, lane = tid & 63;
        const int wid = __builtin_amdgcn_readfirstlane(tid >> 6);
        const unsigned lds0 = (unsigned)(uintptr_t)lds_generic;
        lds_cptr lds = (lds_cptr)lds_generic;
        unsigned vx[NIX], vw[NIW];
#pragma unroll
        for (int i = 0; i < NIX; ++i) { const int j = wid * NIX + i; const int row = 8 * j + (lane >> 3); const int ch = (lane & 7) ^ ((row >> 1) & 7);
            vx[i] = (unsigned)(row * ldx * 2 + ch * 16 - (i & 3) * 1024 + 4096); }
#pragma unroll
        for (int i = 0; i < NIW; ++i) { const int j = wid * NIW + i;
            if (!NN) { const int row = 8 * j + (lane >> 3); const int ch = (lane & 7) ^ ((row >> 1) & 7); vw[i] = (unsigned)(row * ldw * 2 + ch * 16 - (i & 3) * 1024 + 4096); }
            else if (BN == 128) { const int k = 4 * j + (lane >> 4); const int ch = (lane & 15) ^ ((k & 3) << 2); vw[i] = (unsigned)(k * ldw * 2 + ch * 16 - (i & 3) * 1024 + 4096); }
            else if (BN == 64) { const int k = 8 * j + (lane >> 3); const int ch = (lane & 7) ^ (((k >> 1) & 1) << 2); vw[i] = (unsigned)(k * ldw * 2 + ch * 16 - (i & 3) * 1024 + 4096); }
            else if (BN == 32) { const int k = 16 * j + (lane >> 2); const int ch = lane & 3; vw[i] = (unsigned)(k * ldw * 2 + ch * 16 - (i & 3) * 1024 + 4096); }
        }
        const char* xs = (const char*)Xp - 4096; const char* ws = (const char*)Wp - 4096;
        const char* xs2 = (const char*)X2p - 4096; const char* ws2 = (const char*)W2p - 4096;
        const long wstep = NN ? (long)64 * ldw * 2 : 128;
        auto stage = [&](int kt, int slot) {
            const bool s2 = kt >= seg; const int kk = s2 ? kt - seg : kt;
            const char* xb = (s2 ? xs2 : xs) + (long)kk * 128; const char* wb = (s2 ? ws2 : ws) + (long)kk * wstep;
            set_m0(lds0 + slot * SB + wid * NIX * 1024);
            glds16<0>(vx[0], xb); if (NIX > 1) glds16<1024>(vx[NIX > 1 ? 1 : 0], xb); if (NIX > 2) glds16<2048>(vx[NIX > 2 ? 2 : 0], xb); if (NIX > 3) glds16<3072>(vx[NIX > 3 ? 3 : 0], xb);
            if (NIX > 4) { set_m0(lds0 + slot * SB + wid * NIX * 1024 + 4096);
                glds16<0>(vx[NIX > 4 ? 4 : 0], xb); glds16<1024>(vx[NIX > 5 ? 5 : 0], xb); glds16<2048>(vx[NIX > 6 ? 6 : 0], xb); glds16<3072>(vx[NIX > 7 ? 7 : 0], xb); }
            set_m0(lds0 + slot * SB + XB + wid * NIW * 1024);
            glds16<0>(vw[0], wb); if (NIW > 1) glds16<1024>(vw[NIW > 1 ? 1 : 0], wb); if (NIW > 2) glds16<2048>(vw[NIW > 2 ? 2 : 0], wb); if (NIW > 3) glds16<3072>(vw[NIW > 3 ? 3 : 0], wb);
        };
        const int kg = wid / (NWM * NWN), wmn = wid % (NWM * NWN), wm = wmn / NWN, wn = wmn % NWN;
        const int r32 = lane & 31, h = lane >> 5;
        unsigned xa[KS], wa[KS], wt[TN];
        { const int rx = wm * WTM + r32; const unsigned a0 = rx * 128 + ((h ^ ((rx >> 1) & 7)) << 4);
#pragma unroll
          for (int i = 0; i < KS; ++i) xa[i] = a0 ^ ((kg * KS + i) << 5);
          if (!NN) { const int rw = wn * WTN + pi32(r32); const unsigned b0 = XB + rw * 128 + ((h ^ ((rw >> 1) & 7)) << 4);
#pragma unroll
              for (int i = 0; i < KS; ++i) wa[i] = b0 ^ ((kg * KS + i) << 5); } }
        if (NN) { const int g1 = (lane >> 4) & 1, p = lane & 3, q = (lane & 15) >> 2; const int cb = (p & 1) * 16 + g1 * 8 + (p >> 1) * 4;
            const int swz = (BN == 32) ? 0 : (BN == 64) ? ((q >> 1) & 1) : q;
#pragma unroll
            for (int tn = 0; tn < TN; ++tn) { const int c = wn * TN + tn; wt[tn] = XB + (8 * h + q + kg * KS * 16) * (BN * 2) + ((c ^ swz) * 64) + cb * 2; } }
        auto rd = [&](Frag& f, int slot, int i) {
            lds_cptr sb = lds + slot * SB;
            auto rw = [&](int tn) {
                if (!NN) f.w[tn] = *(const LAS bf16x8*)(sb + wa[i] + tn * 32 * 128);
                else { s16x4 lo = __builtin_bit_cast(s16x4, __builtin_amdgcn_ds_read_tr16_b64_v4i16((LAS s16x4*)(sb + wt[tn] + (i * 16) * (BN * 2))));
                       s16x4 hi = __builtin_bit_cast(s16x4, __builtin_amdgcn_ds_read_tr16_b64_v4i16((LAS s16x4*)(sb + wt[tn] + (i * 16 + 4) * (BN * 2))));
                       f.w[tn] = (bf16x8){lo[0], lo[1], lo[2], lo[3], hi[0], hi[1], hi[2], hi[3]}; } };
            rw(0); f.x[0] = *(const LAS bf16x8*)(sb + xa[i]);
#pragma unroll
            for (int tn = 1; tn < TN; ++tn) rw(tn);
#pragma unroll
            for (int tm = 1; tm < TM; ++tm) f.x[tm] = *(const LAS bf16x8*)(sb + xa[i] + tm * 32 * 128);
        };
        auto mm = [&](const Frag& f) {
#pragma unroll
            for (int tm = 0; tm < TM; ++tm)
#pragma unroll
                for (int tn = 0; tn < TN; ++tn) acc[tm][tn] = SWAP ? MFMA32(f.x[tm], f.w[tn], acc[tm][tn]) : MFMA32(f.w[tn], f.x[tm], acc[tm][tn]);
        };
        auto sched = [&]() {
            constexpr int NR = TM + TN * (NN ? 2 : 1), NM = TM * TN;
#pragma unroll
            for (int i = 0; i < NM; ++i) { __builtin_amdgcn_sched_group_barrier(0x8, 1, 0); if (i * 2 < NR) __builtin_amdgcn_sched_group_barrier(0x100, 2, 0); }
        };
        auto bar = [&](auto NOUT) { constexpr int n = decltype(NOUT)::value; __builtin_amdgcn_s_waitcnt(0xC07F); wait_vm<n>(); __builtin_amdgcn_s_barrier(); asm volatile("" ::: "memory"); };
        auto bar_kt = [&](int kt) {
            const int rem = nkt - 1 - kt;
            if (rem >= S - 2) bar(std::integral_constant<int, (S - 2) * NI>{});
            else if (S > 3 && rem == 1) bar(std::integral_constant<int, NI>{});
            else if (S > 4 && rem == 2) bar(std::integral_constant<int, (S > 4 ? 2 * NI : 0)>{});
            else bar(std::integral_constant<int, 0>{});
        };
#pragma unroll
        for (int s = 0; s < S; ++s) stage(s, s);
#pragma unroll
        for (int i = 0; i < TM; ++i)
#pragma unroll
            for (int j = 0; j < TN; ++j) acc[i][j] = f32x16{};
        Frag fa, fb;
        bar(std::integral_constant<int, (S - 1) * NI>{});
        rd(fa, 0, 0);
        int slot = 0;
        if (KS >= 2) {
            auto body = [&](int kt, auto LAST) {
                int nslot = slot + 1; if (nslot == S) nslot = 0;
#pragma unroll
                for (int i = 0; i < KS; i += 2) {
                    rd(fb, slot, i + 1); mm(fa); sched(); SBAR();
                    if (i + 2 < KS) { rd(fa, slot, i + 2); }
                    else if (!decltype(LAST)::value) { bar_kt(kt + 1); if (kt + S < nkt) stage(kt + S, slot); rd(fa, nslot, 0); }
                    mm(fb); sched(); SBAR();
                }
                slot = nslot;
            };
            for (int kt = 0; kt < nkt - 1; ++kt) body(kt, std::false_type{});
            body(nkt - 1, std::true_type{});
        } else {
            auto body = [&](int kt, auto LAST) {
                int nslot = slot + 1; if (nslot == S) nslot = 0;
                bar_kt(kt + 1); if (kt + S < nkt) stage(kt + S, slot); rd(fb, nslot, 0); mm(fa); sched(); SBAR();
                slot = nslot; nslot = slot + 1; if (nslot == S) nslot = 0;
                if (!decltype(LAST)::value) { bar_kt(kt + 2); if (kt + 1 + S < nkt) stage(kt + 1 + S, slot); rd(fa, nslot, 0); }
                mm(fb); sched(); SBAR();
                slot = nslot;
            };
            for (int kt = 0; kt < nkt - 2; kt += 2) body(kt, std::false_type{});
            body(nkt - 2, std::true_type{});
        }
    }
    static DI void reduce(f32x16 (&acc)[TM][TN], char* lds) {
        if (KG == 1) return;
        const int lane = threadIdx.x & 63, wid = threadIdx.x >> 6, kg = wid / (NWM * NWN), wmn = wid % (NWM * NWN);
        lds_bar();
        for (int gq = 1; gq < KG; ++gq) {
            if (kg == gq) {
#pragma unroll
                for (int i = 0; i < TM; ++i)
#pragma unroll
                    for (int j = 0; j < TN; ++j)
#pragma unroll
                        for (int r = 0; r < 4; ++r) *(f32x4*)(lds + ((wmn * TM * TN + i * TN + j) * 4 + r) * 1024 + lane * 16) = (f32x4){acc[i][j][4 * r], acc[i][j][4 * r + 1], acc[i][j][4 * r + 2], acc[i][j][4 * r + 3]};
            }
            lds_bar();
            if (kg == 0) {
#pragma unroll
                for (int i = 0; i < TM; ++i)
#pragma unroll
                    for (int j = 0; j < TN; ++j)
#pragma unroll
                        for (int r = 0; r < 4; ++r) { const f32x4 v = *(const f32x4*)(lds + ((wmn * TM * TN + i * TN + j) * 4 + r) * 1024 + lane * 16);
                            acc[i][j][4 * r] += v[0]; acc[i][j][4 * r + 1] += v[1]; acc[i][j][4 * r + 2] += v[2]; acc[i][j][4 * r + 3] += v[3]; }
            }
            if (gq + 1 < KG) lds_bar();
        }
    }
    static DI void store(const f32x16 (&acc)[TM][TN], u16* out, long ldo, float scale) {
        const int lane = threadIdx.x & 63, wid = threadIdx.x >> 6, kg = wid / (NWM * NWN), wmn = wid % (NWM * NWN), wm = wmn / NWN, wn = wmn % NWN;
        if (kg != 0) return;
#pragma unroll
        for (int i = 0; i < TM; ++i)
#pragma unroll
            for (int j = 0; j < TN; ++j) {
                u16* o = out + (long)(wm * WTM + i * 32 + (lane & 31)) * ldo + wn * WTN + j * 32 + (lane >> 5) * 16;
                const f32x16 a = acc[i][j] * scale;
                *(u32x4*)(o) = (u32x4){cvtpk(a[0], a[1]), cvtpk(a[2], a[3]), cvtpk(a[4], a[5]), cvtpk(a[6], a[7])};
                *(u32x4*)(o + 8) = (u32x4){cvtpk(a[8], a[9]), cvtpk(a[10], a[11]), cvtpk(a[12], a[13]), cvtpk(a[14], a[15])};
            }
    }
};

constexpr int ATT_T = 4, ATT_SB = ATT_T * 4096, ATT_NS = 64 / ATT_T, ATT_LDS = 3 * ATT_SB;
constexpr float ATT_MARGIN = 80.f;
struct PK { u32x4 a, b; };
DI void attn_block(const u16* __restrict__ Q, const char* __restrict__ KV, u16* __restrict__ O, int blk, char* lds_generic) {
    const int tid = threadIdx.x, lane = tid & 63, r32 = lane & 31, h = lane >> 5;
    const int wid = __builtin_amdgcn_readfirstlane(tid >> 6);
    const unsigned lds0 = (unsigned)(uintptr_t)lds_generic;
    lds_cptr lds = (lds_cptr)lds_generic;
    const unsigned voff = lane * 16;
    auto dma4 = [&](int stage, int slot) {
        const char* g = KV + (long)stage * ATT_SB + wid * 4096;
        set_m0(lds0 + slot * ATT_SB + wid * 4096);
        glds16<0>(voff, g); glds16<1024>(voff, g); glds16<2048>(voff, g); glds16<3072>(voff, g);
    };
    dma4(0, 0); dma4(1, 1);
    const int p0 = (blk >> 2) * 32, h0 = (blk & 3) * 8 + wid * 2;
    const u16* qrow = Q + (long)(p0 + r32) * 1024 + h0 * 32 + h * 8;
    const bf16x8 qa0 = *(const bf16x8*)(qrow), qa1 = *(const bf16x8*)(qrow + 16), qb0 = *(const bf16x8*)(qrow + 32), qb1 = *(const bf16x8*)(qrow + 48);
    bf16x8 ones; { const short one = (lane == 0 || lane == 32 || lane == 17 || lane == 49) ? (short)0x3F80 : (short)0; ones = (bf16x8){one, one, one, one, one, one, one, one}; }
    lds_cptr lb = lds + voff;
    auto ldf = [&](int off) { return *(const LAS bf16x8*)(lb + off); };
    wait_vm<4>();
    lds_bar();
    bf16x8 ka0 = ldf(0), ka1 = ldf(1024), kb0 = ldf(4096), kb1 = ldf(4096 + 1024), va0, va1, vb0 = ldf(2048), vb1 = ldf(3072);
    f32x16 SA, SB, negA, negB, OA = {}, OB = {};
    f32x4 LA = {}, LB = {};
    {
        f32x16 z = {};
        SA = MFMA32(ka0, qa0, z); SA = MFMA32(ka1, qa1, SA); SB = MFMA32(ka0, qb0, z); SB = MFMA32(ka1, qb1, SB);
        float ma = SA[0], mb = SB[0];
#pragma unroll
        for (int i = 1; i < 16; ++i) { ma = fmaxf(ma, SA[i]); mb = fmaxf(mb, SB[i]); }
        ma = fmaxf(ma, __shfl_xor(ma, 32)); mb = fmaxf(mb, __shfl_xor(mb, 32));
        const float mA = ma + ATT_MARGIN, mB = mb + ATT_MARGIN;
#pragma unroll
        for (int i = 0; i < 16; ++i) { negA[i] = -mA; negB[i] = -mB; }
        PIN(negA); PIN(negB);
        SA = MFMA32(ka0, qa0, negA); SA = MFMA32(ka1, qa1, SA);
    }
    PK PA, PB; PB.a = (u32x4){0, 0, 0, 0}; PB.b = (u32x4){0, 0, 0, 0};
    va0 = (bf16x8){0, 0, 0, 0, 0, 0, 0, 0}; va1 = va0;
#define EXP(S, i) S[i] = __builtin_amdgcn_exp2f(S[i])
#define CV(P, S, i) P[(i) & 3] = cvtpk(S[2 * (i)], S[2 * (i) + 1])
#define BF(x) __builtin_bit_cast(bf16x8, x)
    auto tile = [&](int koff, int voffs, bf16x8& KC0, bf16x8& KC1, bf16x8& KN0, bf16x8& KN1, bf16x8& VP0, bf16x8& VP1, bf16x8& VC0, bf16x8& VC1, auto LVT) {
        constexpr int LV = decltype(LVT)::value;
        SB = MFMA32(KC0, qb0, negB); SBAR(); EXP(SA, 0); EXP(SA, 1); EXP(SA, 2); EXP(SA, 3); PIN(SA); SBAR();
        SB = MFMA32(KC1, qb1, SB); SBAR(); EXP(SA, 4); EXP(SA, 5); EXP(SA, 6); CV(PA.a, SA, 0); CV(PA.a, SA, 1); PIN(SA); PIN(PA.a); SBAR();
        OB = MFMA32(VP0, BF(PB.a), OB); SBAR(); if (LV == 0) KC0 = ldf(koff); SBAR(); EXP(SA, 7); EXP(SA, 8); EXP(SA, 9); CV(PA.a, SA, 2); CV(PA.a, SA, 3); PIN(SA); PIN(PA.a); SBAR();
        OB = MFMA32(VP1, BF(PB.b), OB); SBAR(); if (LV == 0) KC1 = ldf(koff + 1024); SBAR(); EXP(SA, 10); EXP(SA, 11); EXP(SA, 12); CV(PA.b, SA, 4); CV(PA.b, SA, 5); PIN(SA); PIN(PA.b); SBAR();
        LB = MFMA16(ones, BF(PB.a), LB); SBAR(); EXP(SA, 13); EXP(SA, 14); PIN(SA); SBAR();
        LB = MFMA16(ones, BF(PB.b), LB); SBAR(); EXP(SA, 15); CV(PA.b, SA, 6); CV(PA.b, SA, 7); PIN(PA.b); SBAR();
        if (LV < 2) SA = MFMA32(KN0, qa0, negA); SBAR(); if (LV < 2) VP0 = ldf(voffs); SBAR();
        EXP(SB, 0); EXP(SB, 1); EXP(SB, 2); EXP(SB, 3); PIN(SB); SBAR();
        if (LV < 2) SA = MFMA32(KN1, qa1, SA); SBAR(); if (LV < 2) VP1 = ldf(voffs + 1024); SBAR();
        EXP(SB, 4); EXP(SB, 5); EXP(SB, 6); CV(PB.a, SB, 0); CV(PB.a, SB, 1); PIN(SB); PIN(PB.a); SBAR();
        LA = MFMA16(ones, BF(PA.a), LA); SBAR(); EXP(SB, 7); EXP(SB, 8); PIN(SB); SBAR();
        LA = MFMA16(ones, BF(PA.b), LA); SBAR(); EXP(SB, 9); CV(PB.a, SB, 2); CV(PB.a, SB, 3); PIN(SB); PIN(PB.a); SBAR();
        OA = MFMA32(VC0, BF(PA.a), OA); SBAR(); EXP(SB, 10); EXP(SB, 11); EXP(SB, 12); CV(PB.b, SB, 4); CV(PB.b, SB, 5); PIN(SB); PIN(PB.b); SBAR();
        OA = MFMA32(VC1, BF(PA.b), OA); SBAR(); EXP(SB, 13); EXP(SB, 14); EXP(SB, 15); CV(PB.b, SB, 6); CV(PB.b, SB, 7); PIN(PB.b); SBAR();
    };
    typedef std::integral_constant<int, 0> L0; typedef std::integral_constant<int, 1> L1; typedef std::integral_constant<int, 2> L2;
#define TE(ko, vo, L) tile(ko, vo, ka0, ka1, kb0, kb1, va0, va1, vb0, vb1, L{})
#define TO(ko, vo, L) tile(ko, vo, kb0, kb1, ka0, ka1, vb0, vb1, va0, va1, L{})
    int slot = 0;
    for (int s = 0; s < ATT_NS - 1; ++s) {
        const int cur = slot * ATT_SB; int ns = slot + 1; if (ns == 3) ns = 0; const int nxt = ns * ATT_SB;
#pragma unroll
        for (int j = 0; j < ATT_T - 2; j += 2) { TE(cur + (j + 2) * 4096, cur + (j + 1) * 4096 + 2048, L0); TO(cur + (j + 3) * 4096, cur + (j + 2) * 4096 + 2048, L0); }
        wait_vm<0>();
        asm volatile("" ::: "memory"); __builtin_amdgcn_s_barrier(); asm volatile("" ::: "memory");
        { int fs = ns + 1; if (fs == 3) fs = 0; dma4(s + 2 < ATT_NS ? s + 2 : ATT_NS - 1, fs); }
        TE(nxt, cur + (ATT_T - 1) * 4096 + 2048, L0);
        TO(nxt + 4096, nxt + 2048, L0);
        slot = ns;
    }
    {
        const int cur = slot * ATT_SB;
#pragma unroll
        for (int j = 0; j < ATT_T - 2; j += 2) { TE(cur + (j + 2) * 4096, cur + (j + 1) * 4096 + 2048, L0); TO(cur + (j + 3) * 4096, cur + (j + 2) * 4096 + 2048, L0); }
        TE(0, cur + (ATT_T - 1) * 4096 + 2048, L1);
        TO(0, 0, L2);
        wait_vm<0>();
    }
    OB = MFMA32(va0, BF(PB.a), OB); OB = MFMA32(va1, BF(PB.b), OB); LB = MFMA16(ones, BF(PB.a), LB); LB = MFMA16(ones, BF(PB.b), LB);
    float la, lbv;
    { const float a0 = __shfl(LA[0], lane & 15), a1 = __shfl(LA[1], lane & 15), b0 = __shfl(LB[0], lane & 15), b1 = __shfl(LB[1], lane & 15);
      la = (lane & 16) ? a1 : a0; lbv = (lane & 16) ? b1 : b0; }
    float amax = 0.f, bmax = 0.f; bool bad = !(la < 1e38f) || !(lbv < 1e38f);
#pragma unroll
    for (int i = 0; i < 16; ++i) { bad |= !(fabsf(OA[i]) < 1e38f) | !(fabsf(OB[i]) < 1e38f); amax = fmaxf(amax, fabsf(OA[i])); bmax = fmaxf(bmax, fabsf(OB[i])); }
    bad |= (amax < 1e-30f) | (bmax < 1e-30f);
    if (__builtin_expect(__any(bad), 0)) {
        float ma = -INFINITY, mb = -INFINITY; la = 0.f; lbv = 0.f; OA = f32x16{}; OB = f32x16{};
        for (int t = 0; t < 64; ++t) {
            const char* g = KV + (long)t * 4096 + lane * 16;
            const bf16x8 gk0 = *(const bf16x8*)g, gk1 = *(const bf16x8*)(g + 1024), gv0 = *(const bf16x8*)(g + 2048), gv1 = *(const bf16x8*)(g + 3072);
            f32x16 z = {};
            f32x16 a = MFMA32(gk0, qa0, z); a = MFMA32(gk1, qa1, a); f32x16 b = MFMA32(gk0, qb0, z); b = MFMA32(gk1, qb1, b);
            float ta = a[0], tb = b[0];
#pragma unroll
            for (int i = 1; i < 16; ++i) { ta = fmaxf(ta, a[i]); tb = fmaxf(tb, b[i]); }
            ta = fmaxf(ta, __shfl_xor(ta, 32)); tb = fmaxf(tb, __shfl_xor(tb, 32));
            const float na = fmaxf(ma, ta), nb = fmaxf(mb, tb);
            const float fa = __builtin_amdgcn_exp2f(ma - na), fb = __builtin_amdgcn_exp2f(mb - nb); ma = na; mb = nb;
            la *= fa; lbv *= fb;
            PK pa, pb;
#pragma unroll
            for (int i = 0; i < 16; ++i) { OA[i] *= fa; OB[i] *= fb; a[i] = __builtin_amdgcn_exp2f(a[i] - ma); b[i] = __builtin_amdgcn_exp2f(b[i] - mb); }
#pragma unroll
            for (int i = 0; i < 4; ++i) { pa.a[i] = cvtpk(a[2 * i], a[2 * i + 1]); pa.b[i] = cvtpk(a[8 + 2 * i], a[9 + 2 * i]); pb.a[i] = cvtpk(b[2 * i], b[2 * i + 1]); pb.b[i] = cvtpk(b[8 + 2 * i], b[9 + 2 * i]); }
#pragma unroll
            for (int i = 0; i < 4; ++i) { la += __uint_as_float(pa.a[i] << 16) + __uint_as_float(pa.a[i] & 0xffff0000u) + __uint_as_float(pa.b[i] << 16) + __uint_as_float(pa.b[i] & 0xffff0000u);
                                          lbv += __uint_as_float(pb.a[i] << 16) + __uint_as_float(pb.a[i] & 0xffff0000u) + __uint_as_float(pb.b[i] << 16) + __uint_as_float(pb.b[i] & 0xffff0000u); }
            OA = MFMA32(gv0, BF(pa.a), OA); OA = MFMA32(gv1, BF(pa.b), OA); OB = MFMA32(gv0, BF(pb.a), OB); OB = MFMA32(gv1, BF(pb.b), OB);
        }
        la += __shfl_xor(la, 32); lbv += __shfl_xor(lbv, 32);
    }
    const float ia = 1.f / la, ib = 1.f / lbv;
    u16* orow = O + (long)(p0 + r32) * 1024 + h0 * 32 + h * 16;
    *(u32x4*)(orow) = (u32x4){cvtpk(OA[0] * ia, OA[1] * ia), cvtpk(OA[2] * ia, OA[3] * ia), cvtpk(OA[4] * ia, OA[5] * ia), cvtpk(OA[6] * ia, OA[7] * ia)};
    *(u32x4*)(orow + 8) = (u32x4){cvtpk(OA[8] * ia, OA[9] * ia), cvtpk(OA[10] * ia, OA[11] * ia), cvtpk(OA[12] * ia, OA[13] * ia), cvtpk(OA[14] * ia, OA[15] * ia)};
    *(u32x4*)(orow + 32) = (u32x4){cvtpk(OB[0] * ib, OB[1] * ib), cvtpk(OB[2] * ib, OB[3] * ib), cvtpk(OB[4] * ib, OB[5] * ib), cvtpk(OB[6] * ib, OB[7] * ib)};
    *(u32x4*)(orow + 40) = (u32x4){cvtpk(OB[8] * ib, OB[9] * ib), cvtpk(OB[10] * ib, OB[11] * ib), cvtpk(OB[12] * ib, OB[13] * ib), cvtpk(OB[14] * ib, OB[15] * ib)};
#undef EXP
#undef CV
#undef BF
#undef TE
#undef TO
}

using G128NT = Gemm3<128, 128, 64, 64, 1, 2, false>;
using G128NN = Gemm3<128, 128, 64, 64, 1, 2, true>;
using GKK = Gemm3<128, 32, 32, 32, 1, 3, true, false>;
using GKV = Gemm3<128, 32, 32, 32, 1, 3, true, true>;
using GY = Gemm3<128, 64, 64, 64, 2, 3, false>;
using GWO = Gemm3<128, 64, 64, 64, 2, 3, true>;
constexpr float QSCALE = 0.17677669529663687f * 1.4426950408889634f;

__global__ __launch_bounds__(256, 2) void k1_proj(const u16* __restrict__ xA, const u16* __restrict__ wq, const u16* __restrict__ wk, const u16* __restrict__ wv, const u16* __restrict__ xB,
                                                  const u16* __restrict__ dre, const u16* __restrict__ dim, u16* __restrict__ Q, char* __restrict__ KV, u16* __restrict__ abT) {
    extern __shared__ __attribute__((aligned(16))) char lds[];
    const int b = blockIdx.x, lane = threadIdx.x & 63, wid = threadIdx.x >> 6;
    if (b < 32) {
        const int m0 = (b >> 1) * 128; const int t = (m0 >> 5) + wid;
        if ((b & 1) == 0) {
            f32x16 acc[1][1];
            GKK::run(acc, xA + (long)m0 * 1024, 1024, wk, 32, xA, wk, 16, 16, lds);
            char* dst = KV + (long)t * 4096 + (lane >> 5) * 1024 + (lane & 31) * 16; const f32x16 a = acc[0][0];
            *(u32x4*)(dst) = (u32x4){cvtpk(a[0], a[1]), cvtpk(a[2], a[3]), cvtpk(a[4], a[5]), cvtpk(a[6], a[7])};
            *(u32x4*)(dst + 512) = (u32x4){cvtpk(a[8], a[9]), cvtpk(a[10], a[11]), cvtpk(a[12], a[13]), cvtpk(a[14], a[15])};
        } else {
            f32x16 acc[1][1];
            GKV::run(acc, xA + (long)m0 * 1024, 1024, wv, 32, xA, wv, 16, 16, lds);
            char* dst = KV + (long)t * 4096 + 2048 + lane * 16; const f32x16 a = acc[0][0];
            *(u32x4*)(dst) = (u32x4){cvtpk(a[0], a[1]), cvtpk(a[2], a[3]), cvtpk(a[4], a[5]), cvtpk(a[6], a[7])};
            *(u32x4*)(dst + 1024) = (u32x4){cvtpk(a[8], a[9]), cvtpk(a[10], a[11]), cvtpk(a[12], a[13]), cvtpk(a[14], a[15])};
        }
    } else if (b < 160) {
        const int t = b - 32, g = t & 7, idx = t >> 3;
        const int m0 = ((g >> 1) * 4 + (idx & 3)) * 128, n0 = ((g & 1) * 4 + (idx >> 2)) * 128;
        f32x16 acc[2][2];
        G128NN::run(acc, xA + (long)m0 * 1024, 1024, wq + n0, 1024, xA, wq, 16, 16, lds);
        G128NN::store(acc, Q + (long)m0 * 1024 + n0, 1024, QSCALE);
    } else {
        const int t = b - 160, g = t & 7, idx = t >> 3;
        const int m0 = ((g >> 1) * 4 + (idx & 3)) * 128, n0 = ((g & 1) * 8 + (idx >> 2)) * 128;
        const u16* X = m0 < 1024 ? dre + (long)m0 * 1024 : dim + (long)(m0 - 1024) * 1024;
        f32x16 acc[2][2];
        G128NT::run(acc, X, 1024, xB + (long)n0 * 1024, 1024, X, xB, 16, 16, lds);
        G128NT::store(acc, abT + (long)m0 * 2048 + n0, 2048, m0 < 1024 ? 1.f : -1.f);
    }
}
__global__ __launch_bounds__(256, 2) void k2_attn_y(const u16* __restrict__ Q, const char* __restrict__ KV, u16* __restrict__ O, const u16* __restrict__ sre, const u16* __restrict__ sim,
                                                    const u16* __restrict__ abT, u16* __restrict__ out) {
    extern __shared__ __attribute__((aligned(16))) char lds[];
    const int b = blockIdx.x;
    if (b < 256) { attn_block(Q, KV, O, b, lds); return; }
    const int t = b - 256, g = t & 7, idx = t >> 3;
    const int m0 = ((g >> 1) * 4 + (idx & 3)) * 128, n0 = ((g & 1) * 8 + (idx >> 2)) * 64;
    f32x16 acc[2][2];
    GY::run(acc, sre + (long)m0 * 2048, 2048, abT + (long)n0 * 2048, 2048, sim + (long)m0 * 2048, abT + (long)(1024 + n0) * 2048, 32, 64, lds);
    GY::reduce(acc, lds);
    GY::store(acc, out + (long)m0 * 2048 + 1024 + n0, 2048, 1.f);
}
__global__ __launch_bounds__(256, 2) void k3_wo(const u16* __restrict__ O, const u16* __restrict__ wo, u16* __restrict__ out) {
    extern __shared__ __attribute__((aligned(16))) char lds[];
    const int t = blockIdx.x, g = t & 7, idx = t >> 3;
    const int m0 = ((g >> 1) * 4 + (idx & 3)) * 128, n0 = ((g & 1) * 8 + (idx >> 2)) * 64;
    f32x16 acc[2][2];
    GWO::run(acc, O + (long)m0 * 1024, 1024, wo + n0, 1024, O, wo, 16, 16, lds);
    GWO::reduce(acc, lds);
    GWO::store(acc, out + (long)m0 * 2048 + n0, 2048, 1.f);
}

extern "C" void kernel_launch(void* const* d_in, const int* in_sizes, int n_in, void* d_out, int out_size, void* d_ws, size_t ws_size, hipStream_t stream) {
    const u16* xA = (const u16*)d_in[0]; const u16* wq = (const u16*)d_in[1]; const u16* wk = (const u16*)d_in[2]; const u16* wv = (const u16*)d_in[3]; const u16* wo = (const u16*)d_in[4];
    const u16* xB = (const u16*)d_in[5]; const u16* sre = (const u16*)d_in[6]; const u16* sim = (const u16*)d_in[7]; const u16* dre = (const u16*)d_in[8]; const u16* dim = (const u16*)d_in[9];
    u16* out = (u16*)d_out; char* ws = (char*)d_ws;
    u16* Q = (u16*)ws; u16* O = (u16*)(ws + (4 << 20)); u16* abT = (u16*)(ws + (8 << 20)); char* KV = ws + (16 << 20);
    constexpr int L1 = 65536, L2 = 73728, L3 = 73728;
    hipFuncSetAttribute((const void*)k1_proj, hipFuncAttributeMaxDynamicSharedMemorySize, L1);
    hipFuncSetAttribute((const void*)k2_attn_y, hipFuncAttributeMaxDynamicSharedMemorySize, L2);
    hipFuncSetAttribute((const void*)k3_wo, hipFuncAttributeMaxDynamicSharedMemorySize, L3);
    k1_proj<<<416, 256, L1, stream>>>(xA, wq, wk, wv, xB, dre, dim, Q, KV, abT);
    k2_attn_y<<<512, 256, L2, stream>>>(Q, KV, O, sre, sim, abT, out);
    k3_wo<<<256, 256, L3, stream>>>(O, wo, out);
}
```

```cpp
#include <hip/hip_runtime.h>
#include <hip/hip_bf16.h>
#include <type_traits>
typedef unsigned short u16;
typedef short bf16x8 __attribute__((ext_vector_type(8)));
typedef short s16x4 __attribute__((ext_vector_type(4)));
typedef float f32x16 __attribute__((ext_vector_type(16)));
typedef float f32x4 __attribute__((ext_vector_type(4)));
typedef float f32x2 __attribute__((ext_vector_type(2)));
typedef unsigned u32x4 __attribute__((ext_vector_type(4)));
typedef __bf16 bf16x2_t __attribute__((ext_vector_type(2)));
#define LAS __attribute__((address_space(3)))
typedef LAS const char* lds_cptr;
#define MFMA32(a, b, c) __builtin_amdgcn_mfma_f32_32x32x16_bf16(a, b, c, 0, 0, 0)
#define MFMA16(a, b, c) __builtin_amdgcn_mfma_f32_16x16x32_bf16(a, b, c, 0, 0, 0)
#define SBAR() __builtin_amdgcn_sched_barrier(0)
#define PIN(x) asm volatile("" : "+v"(x))
#define DI __device__ __forceinline__
#define CODEPF 1
#define K2_TAIL 40
#define K3_LINES 57
#define ST16(p, v) (*(u32x4*)(p) = (v))
DI unsigned cvtpk(float lo, float hi) { f32x2 v = {lo, hi}; bf16x2_t r = __builtin_convertvector(v, bf16x2_t); return __builtin_bit_cast(unsigned, r); }
template <int OFF> DI void glds16(unsigned voff, const void* sbase) {
    asm volatile("global_load_lds_dwordx4 %0, %1 offset:%c2" ::"v"(voff), "s"(sbase), "i"(OFF) : "memory");
}
DI void set_m0(unsigned v) { asm volatile("s_mov_b32 m0, %0\n\ts_nop 0" ::"s"(v) : "memory"); }
template <int N> DI void wait_vm() { asm volatile("s_waitcnt vmcnt(%c0)" ::"i"(N) : "memory"); }
DI void lds_bar() { __builtin_amdgcn_s_waitcnt(0xC07F); asm volatile("" ::: "memory"); __builtin_amdgcn_s_barrier(); asm volatile("" ::: "memory"); }
DI int pi32(int r) { return ((r >> 2) & 1) * 16 + ((r >> 4) & 1) * 8 + ((r >> 3) & 1) * 4 + (r & 3); }

template <int BM, int BN, int WTM, int WTN, int KG, int S, bool NN, bool SWAP = false>
struct Gemm3 {
    static constexpr int NWM = BM / WTM, NWN = BN / WTN, NW = NWM * NWN * KG;
    static_assert(NW == 4, "4 waves");
    static constexpr int XB = BM * 128, WB = BN * 128, SB = XB + WB;
    static constexpr int NIX = (BM / 8) / NW, NIW = (BN / 8) / NW, NI = NIX + NIW;
    static constexpr int TM = WTM / 32, TN = WTN / 32, KS = 4 / KG;
    static constexpr int LDS_BYTES = S * SB;
    static_assert(NIX <= 8 && NIW <= 4 && NIW >= 1 && (S - 1) * NI <= 63, "");
    struct Frag { bf16x8 x[TM]; bf16x8 w[TN]; };

    static DI void run(f32x16 (&acc)[TM][TN], const u16* Xp, int ldx, const u16* Wp, int ldw, const u16* X2p, const u16* W2p, int seg, int nkt, char* lds_generic) {
        const int tid = threadIdx.x, lane = tid & 63;
        const int wid = __builtin_amdgcn_readfirstlane(tid >> 6);
        const unsigned lds0 = (unsigned)(uintptr_t)lds_generic;
        lds_cptr lds = (lds_cptr)lds_generic;
        unsigned vx[NIX], vw[NIW];
#pragma unroll
        for (int i = 0; i < NIX; ++i) { const int j = wid * NIX + i; const int row = 8 * j + (lane >> 3); const int ch = (lane & 7) ^ ((row >> 1) & 7);
            vx[i] = (unsigned)(row * ldx * 2 + ch * 16 - (i & 3) * 1024 + 4096); }
#pragma unroll
        for (int i = 0; i < NIW; ++i) { const int j = wid * NIW + i;
            if (!NN) { const int row = 8 * j + (lane >> 3); const int ch = (lane & 7) ^ ((row >> 1) & 7); vw[i] = (unsigned)(row * ldw * 2 + ch * 16 - (i & 3) * 1024 + 4096); }
            else if (BN == 128) { const int k = 4 * j + (lane >> 4); const int ch = (lane & 15) ^ ((k & 3) << 2); vw[i] = (unsigned)(k * ldw * 2 + ch * 16 - (i & 3) * 1024 + 4096); }
            else if (BN == 64) { const int k = 8 * j + (lane >> 3); const int ch = (lane & 7) ^ (((k >> 1) & 1) << 2); vw[i] = (unsigned)(k * ldw * 2 + ch * 16 - (i & 3) * 1024 + 4096); }
            else if (BN == 32) { const int k = 16 * j + (lane >> 2); const int ch = lane & 3; vw[i] = (unsigned)(k * ldw * 2 + ch * 16 - (i & 3) * 1024 + 4096); }
        }
        const char* xs = (const char*)Xp - 4096; const char* ws = (const char*)Wp - 4096;
        const char* xs2 = (const char*)X2p - 4096; const char* ws2 = (const char*)W2p - 4096;
        const long wstep = NN ? (long)64 * ldw * 2 : 128;
        auto stage = [&](int kt, int slot) {
            const bool s2 = kt >= seg; const int kk = s2 ? kt - seg : kt;
            const char* xb = (s2 ? xs2 : xs) + (long)kk * 128; const char* wb = (s2 ? ws2 : ws) + (long)kk * wstep;
            set_m0(lds0 + slot * SB + wid * NIX * 1024);
            glds16<0>(vx[0], xb); if (NIX > 1) glds16<1024>(vx[NIX > 1 ? 1 : 0], xb); if (NIX > 2) glds16<2048>(vx[NIX > 2 ? 2 : 0], xb); if (NIX > 3) glds16<3072>(vx[NIX > 3 ? 3 : 0], xb);
            if (NIX > 4) { set_m0(lds0 + slot * SB + wid * NIX * 1024 + 4096);
                glds16<0>(vx[NIX > 4 ? 4 : 0], xb); glds16<1024>(vx[NIX > 5 ? 5 : 0], xb); glds16<2048>(vx[NIX > 6 ? 6 : 0], xb); glds16<3072>(vx[NIX > 7 ? 7 : 0], xb); }
            set_m0(lds0 + slot * SB + XB + wid * NIW * 1024);
            glds16<0>(vw[0], wb); if (NIW > 1) glds16<1024>(vw[NIW > 1 ? 1 : 0], wb); if (NIW > 2) glds16<2048>(vw[NIW > 2 ? 2 : 0], wb); if (NIW > 3) glds16<3072>(vw[NIW > 3 ? 3 : 0], wb);
        };
        const int kg = wid / (NWM * NWN), wmn = wid % (NWM * NWN), wm = wmn / NWN, wn = wmn % NWN;
        const int r32 = lane & 31, h = lane >> 5;
        unsigned xa[KS], wa[KS], wt[TN];
        { const int rx = wm * WTM + r32; const unsigned a0 = rx * 128 + ((h ^ ((rx >> 1) & 7)) << 4);
#pragma unroll
          for (int i = 0; i < KS; ++i) xa[i] = a0 ^ ((kg * KS + i) << 5);
          if (!NN) { const int rw = wn * WTN + pi32(r32); const unsigned b0 = XB + rw * 128 + ((h ^ ((rw >> 1) & 7)) << 4);
#pragma unroll
              for (int i = 0; i < KS; ++i) wa[i] = b0 ^ ((kg * KS + i) << 5); } }
        if (NN) { const int g1 = (lane >> 4) & 1, p = lane & 3, q = (lane & 15) >> 2; const int cb = (p & 1) * 16 + g1 * 8 + (p >> 1) * 4;
            const int swz = (BN == 32) ? 0 : (BN == 64) ? ((q >> 1) & 1) : q;
#pragma unroll
            for (int tn = 0; tn < TN; ++tn) { const int c = wn * TN + tn; wt[tn] = XB + (8 * h + q + kg * KS * 16) * (BN * 2) + ((c ^ swz) * 64) + cb * 2; } }
        auto rd = [&](Frag& f, int slot, int i) {
            lds_cptr sb = lds + slot * SB;
            auto rw = [&](int tn) {
                if (!NN) f.w[tn] = *(const LAS bf16x8*)(sb + wa[i] + tn * 32 * 128);
                else { s16x4 lo = __builtin_bit_cast(s16x4, __builtin_amdgcn_ds_read_tr16_b64_v4i16((LAS s16x4*)(sb + wt[tn] + (i * 16) * (BN * 2))));
                       s16x4 hi = __builtin_bit_cast(s16x4, __builtin_amdgcn_ds_read_tr16_b64_v4i16((LAS s16x4*)(sb + wt[tn] + (i * 16 + 4) * (BN * 2))));
                       f.w[tn] = (bf16x8){lo[0], lo[1], lo[2], lo[3], hi[0], hi[1], hi[2], hi[3]}; } };
            rw(0); f.x[0] = *(const LAS bf16x8*)(sb + xa[i]);
#pragma unroll
            for (int tn = 1; tn < TN; ++tn) rw(tn);
#pragma unroll
            for (int tm = 1; tm < TM; ++tm) f.x[tm] = *(const LAS bf16x8*)(sb + xa[i] + tm * 32 * 128);
        };
        auto mm = [&](const Frag& f) {
#pragma unroll
            for (int tm = 0; tm < TM; ++tm)
#pragma unroll
                for (int tn = 0; tn < TN; ++tn) acc[tm][tn] = SWAP ? MFMA32(f.x[tm], f.w[tn], acc[tm][tn]) : MFMA32(f.w[tn], f.x[tm], acc[tm][tn]);
        };
        auto sched = [&]() {
            constexpr int NR = TM + TN * (NN ? 2 : 1), NM = TM * TN;
#pragma unroll
            for (int i = 0; i < NM; ++i) { __builtin_amdgcn_sched_group_barrier(0x8, 1, 0); if (i * 2 < NR) __builtin_amdgcn_sched_group_barrier(0x100, 2, 0); }
        };
        auto bar = [&](auto NOUT) { constexpr int n = decltype(NOUT)::value; __builtin_amdgcn_s_waitcnt(0xC07F); wait_vm<n>(); __builtin_amdgcn_s_barrier(); asm volatile("" ::: "memory"); };
        auto bar_kt = [&](int kt) {
            const int rem = nkt - 1 - kt;
            if (rem >= S - 2) bar(std::integral_constant<int, (S - 2) * NI>{});
            else if (S > 3 && rem == 1) bar(std::integral_constant<int, NI>{});
            else if (S > 4 && rem == 2) bar(std::integral_constant<int, (S > 4 ? 2 * NI : 0)>{});
            else bar(std::integral_constant<int, 0>{});
        };
#pragma unroll
        for (int s = 0; s < S; ++s) stage(s, s);
#pragma unroll
        for (int i = 0; i < TM; ++i)
#pragma unroll
            for (int j = 0; j < TN; ++j) acc[i][j] = f32x16{};
        Frag fa, fb;
        bar(std::integral_constant<int, (S - 1) * NI>{});
        rd(fa, 0, 0);
        int slot = 0;
        if (KS >= 2) {
            auto body = [&](int kt, auto LAST) {
                int nslot = slot + 1; if (nslot == S) nslot = 0;
#pragma unroll
                for (int i = 0; i < KS; i += 2) {
                    rd(fb, slot, i + 1); mm(fa); sched(); SBAR();
                    if (i + 2 < KS) { rd(fa, slot, i + 2); }
                    else if (!decltype(LAST)::value) { bar_kt(kt + 1); if (kt + S < nkt) stage(kt + S, slot); rd(fa, nslot, 0); }
                    mm(fb); sched(); SBAR();
                }
                slot = nslot;
            };
            for (int kt = 0; kt < nkt - 1; ++kt) body(kt, std::false_type{});
            body(nkt - 1, std::true_type{});
        } else {
            auto body = [&](int kt, auto LAST) {
                int nslot = slot + 1; if (nslot == S) nslot = 0;
                bar_kt(kt + 1); if (kt + S < nkt) stage(kt + S, slot); rd(fb, nslot, 0); mm(fa); sched(); SBAR();
                slot = nslot; nslot = slot + 1; if (nslot == S) nslot = 0;
                if (!decltype(LAST)::value) { bar_kt(kt + 2); if (kt + 1 + S < nkt) stage(kt + 1 + S, slot); rd(fa, nslot, 0); }
                mm(fb); sched(); SBAR();
                slot = nslot;
            };
            for (int kt = 0; kt < nkt - 2; kt += 2) body(kt, std::false_type{});
            body(nkt - 2, std::true_type{});
        }
    }
    static DI void reduce(f32x16 (&acc)[TM][TN], char* lds) {
        if (KG == 1) return;
        const int lane = threadIdx.x & 63, wid = threadIdx.x >> 6, kg = wid / (NWM * NWN), wmn = wid % (NWM * NWN);
        lds_bar();
        for (int gq = 1; gq < KG; ++gq) {
            if (kg == gq) {
#pragma unroll
                for (int i = 0; i < TM; ++i)
#pragma unroll
                    for (int j = 0; j < TN; ++j)
#pragma unroll
                        for (int r = 0; r < 4; ++r) *(f32x4*)(lds + ((wmn * TM * TN + i * TN + j) * 4 + r) * 1024 + lane * 16) = (f32x4){acc[i][j][4 * r], acc[i][j][4 * r + 1], acc[i][j][4 * r + 2], acc[i][j][4 * r + 3]};
            }
            lds_bar();
            if (kg == 0) {
#pragma unroll
                for (int i = 0; i < TM; ++i)
#pragma unroll
                    for (int j = 0; j < TN; ++j)
#pragma unroll
                        for (int r = 0; r < 4; ++r) { const f32x4 v = *(const f32x4*)(lds + ((wmn * TM * TN + i * TN + j) * 4 + r) * 1024 + lane * 16);
                            acc[i][j][4 * r] += v[0]; acc[i][j][4 * r + 1] += v[1]; acc[i][j][4 * r + 2] += v[2]; acc[i][j][4 * r + 3] += v[3]; }
            }
            if (gq + 1 < KG) lds_bar();
        }
    }
    static DI void store(const f32x16 (&acc)[TM][TN], u16* out, long ldo, float scale) {
        const int lane = threadIdx.x & 63, wid = threadIdx.x >> 6, kg = wid / (NWM * NWN), wmn = wid % (NWM * NWN), wm = wmn / NWN, wn = wmn % NWN;
        if (kg != 0) return;
#pragma unroll
        for (int i = 0; i < TM; ++i)
#pragma unroll
            for (int j = 0; j < TN; ++j) {
                u16* o = out + (long)(wm * WTM + i * 32 + (lane & 31)) * ldo + wn * WTN + j * 32 + (lane >> 5) * 16;
                const f32x16 a = acc[i][j] * scale;
                *(u32x4*)(o) = (u32x4){cvtpk(a[0], a[1]), cvtpk(a[2], a[3]), cvtpk(a[4], a[5]), cvtpk(a[6], a[7])};
                *(u32x4*)(o + 8) = (u32x4){cvtpk(a[8], a[9]), cvtpk(a[10], a[11]), cvtpk(a[12], a[13]), cvtpk(a[14], a[15])};
            }
    }
};

DI void kb_256x128_nt3(f32x16 (&acc)[4][2], const unsigned (&xa)[4], const unsigned (&wa)[4], int n, int sn) {
    unsigned dl;
    asm volatile(
        "s_nop 4\n"
        "v_mov_b32 v208, %[xa0]\n"
        "v_mov_b32 v209, %[xa1]\n"
        "v_mov_b32 v210, %[xa2]\n"
        "v_mov_b32 v211, %[xa3]\n"
        "v_mov_b32 v212, %[wa0]\n"
        "v_mov_b32 v213, %[wa1]\n"
        "v_mov_b32 v214, %[wa2]\n"
        "v_mov_b32 v215, %[wa3]\n"
        "ds_read_b128 v[160:163], v212 offset:0\n"
        "ds_read_b128 v[168:171], v208 offset:0\n"
        "ds_read_b128 v[164:167], v212 offset:4096\n"
        "ds_read_b128 v[172:175], v208 offset:4096\n"
        "ds_read_b128 v[176:179], v208 offset:8192\n"
        "ds_read_b128 v[180:183], v208 offset:12288\n"
        "Lloop%=:\n"
        "s_waitcnt lgkmcnt(0)\n"
        "v_mfma_f32_32x32x16_bf16 v[32:47], v[160:163], v[168:171], v[32:47]\n"
        "ds_read_b128 v[184:187], v213 offset:0\n"
        "ds_read_b128 v[192:195], v209 offset:0\n"
        "s_cmp_eq_u32 %[sn], 0\n"
        "s_cselect_b32 %[dl], %[dm], %[dp]\n"
        "v_mfma_f32_32x32x16_bf16 v[48:63], v[164:167], v[168:171], v[48:63]\n"
        "ds_read_b128 v[188:191], v213 offset:4096\n"
        "ds_read_b128 v[196:199], v209 offset:4096\n"
        "v_mfma_f32_32x32x16_bf16 v[64:79], v[160:163], v[172:175], v[64:79]\n"
        "ds_read_b128 v[200:203], v209 offset:8192\n"
        "ds_read_b128 v[204:207], v209 offset:12288\n"
        "v_mfma_f32_32x32x16_bf16 v[80:95], v[164:167], v[172:175], v[80:95]\n"
        "v_mfma_f32_32x32x16_bf16 v[96:111], v[160:163], v[176:179], v[96:111]\n"
        "v_mfma_f32_32x32x16_bf16 v[112:127], v[164:167], v[176:179], v[112:127]\n"
        "v_mfma_f32_32x32x16_bf16 v[128:143], v[160:163], v[180:183], v[128:143]\n"
        "v_add_u32 v212, %[dl], v212\n"
        "v_mfma_f32_32x32x16_bf16 v[144:159], v[164:167], v[180:183], v[144:159]\n"
        "v_add_u32 v208, %[dl], v208\n"
        "s_waitcnt lgkmcnt(0)\n"
        "v_mfma_f32_32x32x16_bf16 v[32:47], v[184:187], v[192:195], v[32:47]\n"
        "ds_read_b128 v[160:163], v214 offset:0\n"
        "ds_read_b128 v[168:171], v210 offset:0\n"
        "v_add_u32 v209, %[dl], v209\n"
        "v_mfma_f32_32x32x16_bf16 v[48:63], v[188:191], v[192:195], v[48:63]\n"
        "ds_read_b128 v[164:167], v214 offset:4096\n"
        "ds_read_b128 v[172:175], v210 offset:4096\n"
        "v_add_u32 v213, %[dl], v213\n"
        "v_mfma_f32_32x32x16_bf16 v[64:79], v[184:187], v[196:199], v[64:79]\n"
        "ds_read_b128 v[176:179], v210 offset:8192\n"
        "ds_read_b128 v[180:183], v210 offset:12288\n"
        "v_mfma_f32_32x32x16_bf16 v[80:95], v[188:191], v[196:199], v[80:95]\n"
        "v_mfma_f32_32x32x16_bf16 v[96:111], v[184:187], v[200:203], v[96:111]\n"
        "v_mfma_f32_32x32x16_bf16 v[112:127], v[188:191], v[200:203], v[112:127]\n"
        "v_mfma_f32_32x32x16_bf16 v[128:143], v[184:187], v[204:207], v[128:143]\n"
        "v_mfma_f32_32x32x16_bf16 v[144:159], v[188:191], v[204:207], v[144:159]\n"
        "s_waitcnt lgkmcnt(0)\n"
        "v_mfma_f32_32x32x16_bf16 v[32:47], v[160:163], v[168:171], v[32:47]\n"
        "ds_read_b128 v[184:187], v215 offset:0\n"
        "ds_read_b128 v[192:195], v211 offset:0\n"
        "v_add_u32 v210, %[dl], v210\n"
        "v_mfma_f32_32x32x16_bf16 v[48:63], v[164:167], v[168:171], v[48:63]\n"
        "ds_read_b128 v[188:191], v215 offset:4096\n"
        "ds_read_b128 v[196:199], v211 offset:4096\n"
        "v_add_u32 v214, %[dl], v214\n"
        "v_mfma_f32_32x32x16_bf16 v[64:79], v[160:163], v[172:175], v[64:79]\n"
        "ds_read_b128 v[200:203], v211 offset:8192\n"
        "ds_read_b128 v[204:207], v211 offset:12288\n"
        "v_mfma_f32_32x32x16_bf16 v[80:95], v[164:167], v[172:175], v[80:95]\n"
        "v_mfma_f32_32x32x16_bf16 v[96:111], v[160:163], v[176:179], v[96:111]\n"
        "v_mfma_f32_32x32x16_bf16 v[112:127], v[164:167], v[176:179], v[112:127]\n"
        "v_mfma_f32_32x32x16_bf16 v[128:143], v[160:163], v[180:183], v[128:143]\n"
        "v_mfma_f32_32x32x16_bf16 v[144:159], v[164:167], v[180:183], v[144:159]\n"
        "s_waitcnt lgkmcnt(0)\n"
        "s_barrier\n"
        "v_mfma_f32_32x32x16_bf16 v[32:47], v[184:187], v[192:195], v[32:47]\n"
        "ds_read_b128 v[160:163], v212 offset:0\n"
        "ds_read_b128 v[168:171], v208 offset:0\n"
        "v_add_u32 v211, %[dl], v211\n"
        "v_mfma_f32_32x32x16_bf16 v[48:63], v[188:191], v[192:195], v[48:63]\n"
        "ds_read_b128 v[164:167], v212 offset:4096\n"
        "ds_read_b128 v[172:175], v208 offset:4096\n"
        "v_add_u32 v215, %[dl], v215\n"
        "v_mfma_f32_32x32x16_bf16 v[64:79], v[184:187], v[196:199], v[64:79]\n"
        "ds_read_b128 v[176:179], v208 offset:8192\n"
        "ds_read_b128 v[180:183], v208 offset:12288\n"
        "v_mfma_f32_32x32x16_bf16 v[80:95], v[188:191], v[196:199], v[80:95]\n"
        "v_mfma_f32_32x32x16_bf16 v[96:111], v[184:187], v[200:203], v[96:111]\n"
        "s_add_i32 %[sn], %[sn], 1\n"
        "s_cmp_eq_u32 %[sn], 3\n"
        "s_cselect_b32 %[sn], 0, %[sn]\n"
        "v_mfma_f32_32x32x16_bf16 v[112:127], v[188:191], v[200:203], v[112:127]\n"
        "v_mfma_f32_32x32x16_bf16 v[128:143], v[184:187], v[204:207], v[128:143]\n"
        "s_sub_i32 %[n], %[n], 1\n"
        "v_mfma_f32_32x32x16_bf16 v[144:159], v[188:191], v[204:207], v[144:159]\n"
        "s_cmp_lg_u32 %[n], 0\n"
        "s_cbranch_scc1 Lloop%=\n"
        "s_waitcnt lgkmcnt(0)\n"
        "s_nop 15\n"
        "s_nop 3\n"
        : "+{v[32:47]}"(acc[0][0]), "+{v[48:63]}"(acc[0][1]), "+{v[64:79]}"(acc[1][0]), "+{v[80:95]}"(acc[1][1]), "+{v[96:111]}"(acc[2][0]), "+{v[112:127]}"(acc[2][1]), "+{v[128:143]}"(acc[3][0]), "+{v[144:159]}"(acc[3][1]), [n] "+s"(n), [sn] "+s"(sn), [dl] "=&s"(dl)
        : [xa0] "v"(xa[0]), [xa1] "v"(xa[1]), [xa2] "v"(xa[2]), [xa3] "v"(xa[3]), [wa0] "v"(wa[0]), [wa1] "v"(wa[1]), [wa2] "v"(wa[2]), [wa3] "v"(wa[3]), [dm] "s"(-98304), [dp] "s"(49152)
        : "memory", "scc", "v160", "v161", "v162", "v163", "v164", "v165", "v166", "v167", "v168", "v169", "v170", "v171", "v172", "v173", "v174", "v175", "v176", "v177", "v178", "v179", "v180", "v181", "v182", "v183", "v184", "v185", "v186", "v187", "v188", "v189", "v190", "v191", "v192", "v193", "v194", "v195", "v196", "v197", "v198", "v199", "v200", "v201", "v202", "v203", "v204", "v205", "v206", "v207", "v208", "v209", "v210", "v211", "v212", "v213", "v214", "v215");
}
DI void kb_256x128_nn3(f32x16 (&acc)[4][2], const unsigned (&xa)[4], const unsigned (&wa)[2], int n, int sn) {
    unsigned dl;
    asm volatile(
        "s_nop 4\n"
        "v_mov_b32 v208, %[xa0]\n"
        "v_mov_b32 v209, %[xa1]\n"
        "v_mov_b32 v210, %[xa2]\n"
        "v_mov_b32 v211, %[xa3]\n"
        "v_mov_b32 v212, %[wa0]\n"
        "v_mov_b32 v213, %[wa1]\n"
        "ds_read_b64_tr_b16 v[160:161], v212 offset:0\n"
        "ds_read_b64_tr_b16 v[162:163], v212 offset:1024\n"
        "ds_read_b128 v[168:171], v208 offset:0\n"
        "ds_read_b64_tr_b16 v[164:165], v213 offset:0\n"
        "ds_read_b64_tr_b16 v[166:167], v213 offset:1024\n"
        "ds_read_b128 v[172:175], v208 offset:4096\n"
        "ds_read_b128 v[176:179], v208 offset:8192\n"
        "ds_read_b128 v[180:183], v208 offset:12288\n"
        "Lloop%=:\n"
        "s_waitcnt lgkmcnt(0)\n"
        "v_mfma_f32_32x32x16_bf16 v[32:47], v[160:163], v[168:171], v[32:47]\n"
        "ds_read_b64_tr_b16 v[184:185], v212 offset:4096\n"
        "ds_read_b64_tr_b16 v[186:187], v212 offset:5120\n"
        "s_cmp_eq_u32 %[sn], 0\n"
        "s_cselect_b32 %[dl], %[dm], %[dp]\n"
        "v_mfma_f32_32x32x16_bf16 v[48:63], v[164:167], v[168:171], v[48:63]\n"
        "ds_read_b128 v[192:195], v209 offset:0\n"
        "ds_read_b64_tr_b16 v[188:189], v213 offset:4096\n"
        "v_mfma_f32_32x32x16_bf16 v[64:79], v[160:163], v[172:175], v[64:79]\n"
        "ds_read_b64_tr_b16 v[190:191], v213 offset:5120\n"
        "ds_read_b128 v[196:199], v209 offset:4096\n"
        "v_mfma_f32_32x32x16_bf16 v[80:95], v[164:167], v[172:175], v[80:95]\n"
        "ds_read_b128 v[200:203], v209 offset:8192\n"
        "ds_read_b128 v[204:207], v209 offset:12288\n"
        "v_mfma_f32_32x32x16_bf16 v[96:111], v[160:163], v[176:179], v[96:111]\n"
        "v_mfma_f32_32x32x16_bf16 v[112:127], v[164:167], v[176:179], v[112:127]\n"
        "v_mfma_f32_32x32x16_bf16 v[128:143], v[160:163], v[180:183], v[128:143]\n"
        "v_mfma_f32_32x32x16_bf16 v[144:159], v[164:167], v[180:183], v[144:159]\n"
        "v_add_u32 v208, %[dl], v208\n"
        "s_waitcnt lgkmcnt(0)\n"
        "v_mfma_f32_32x32x16_bf16 v[32:47], v[184:187], v[192:195], v[32:47]\n"
        "ds_read_b64_tr_b16 v[160:161], v212 offset:8192\n"
        "ds_read_b64_tr_b16 v[162:163], v212 offset:9216\n"
        "v_add_u32 v209, %[dl], v209\n"
        "v_mfma_f32_32x32x16_bf16 v[48:63], v[188:191], v[192:195], v[48:63]\n"
        "ds_read_b128 v[168:171], v210 offset:0\n"
        "ds_read_b64_tr_b16 v[164:165], v213 offset:8192\n"
        "v_mfma_f32_32x32x16_bf16 v[64:79], v[184:187], v[196:199], v[64:79]\n"
        "ds_read_b64_tr_b16 v[166:167], v213 offset:9216\n"
        "ds_read_b128 v[172:175], v210 offset:4096\n"
        "v_mfma_f32_32x32x16_bf16 v[80:95], v[188:191], v[196:199], v[80:95]\n"
        "ds_read_b128 v[176:179], v210 offset:8192\n"
        "ds_read_b128 v[180:183], v210 offset:12288\n"
        "v_mfma_f32_32x32x16_bf16 v[96:111], v[184:187], v[200:203], v[96:111]\n"
        "v_mfma_f32_32x32x16_bf16 v[112:127], v[188:191], v[200:203], v[112:127]\n"
        "v_mfma_f32_32x32x16_bf16 v[128:143], v[184:187], v[204:207], v[128:143]\n"
        "v_mfma_f32_32x32x16_bf16 v[144:159], v[188:191], v[204:207], v[144:159]\n"
        "s_waitcnt lgkmcnt(0)\n"
        "v_mfma_f32_32x32x16_bf16 v[32:47], v[160:163], v[168:171], v[32:47]\n"
        "ds_read_b64_tr_b16 v[184:185], v212 offset:12288\n"
        "ds_read_b64_tr_b16 v[186:187], v212 offset:13312\n"
        "v_add_u32 v210, %[dl], v210\n"
        "v_mfma_f32_32x32x16_bf16 v[48:63], v[164:167], v[168:171], v[48:63]\n"
        "ds_read_b128 v[192:195], v211 offset:0\n"
        "ds_read_b64_tr_b16 v[188:189], v213 offset:12288\n"
        "v_mfma_f32_32x32x16_bf16 v[64:79], v[160:163], v[172:175], v[64:79]\n"
        "ds_read_b64_tr_b16 v[190:191], v213 offset:13312\n"
        "ds_read_b128 v[196:199], v211 offset:4096\n"
        "v_mfma_f32_32x32x16_bf16 v[80:95], v[164:167], v[172:175], v[80:95]\n"
        "ds_read_b128 v[200:203], v211 offset:8192\n"
        "ds_read_b128 v[204:207], v211 offset:12288\n"
        "v_mfma_f32_32x32x16_bf16 v[96:111], v[160:163], v[176:179], v[96:111]\n"
        "v_mfma_f32_32x32x16_bf16 v[112:127], v[164:167], v[176:179], v[112:127]\n"
        "v_mfma_f32_32x32x16_bf16 v[128:143], v[160:163], v[180:183], v[128:143]\n"
        "v_mfma_f32_32x32x16_bf16 v[144:159], v[164:167], v[180:183], v[144:159]\n"
        "v_add_u32 v212, %[dl], v212\n"
        "v_add_u32 v213, %[dl], v213\n"
        "s_waitcnt lgkmcnt(0)\n"
        "s_barrier\n"
        "v_mfma_f32_32x32x16_bf16 v[32:47], v[184:187], v[192:195], v[32:47]\n"
        "ds_read_b64_tr_b16 v[160:161], v212 offset:0\n"
        "ds_read_b64_tr_b16 v[162:163], v212 offset:1024\n"
        "v_add_u32 v211, %[dl], v211\n"
        "v_mfma_f32_32x32x16_bf16 v[48:63], v[188:191], v[192:195], v[48:63]\n"
        "ds_read_b128 v[168:171], v208 offset:0\n"
        "ds_read_b64_tr_b16 v[164:165], v213 offset:0\n"
        "v_mfma_f32_32x32x16_bf16 v[64:79], v[184:187], v[196:199], v[64:79]\n"
        "ds_read_b64_tr_b16 v[166:167], v213 offset:1024\n"
        "ds_read_b128 v[172:175], v208 offset:4096\n"
        "v_mfma_f32_32x32x16_bf16 v[80:95], v[188:191], v[196:199], v[80:95]\n"
        "ds_read_b128 v[176:179], v208 offset:8192\n"
        "ds_read_b128 v[180:183], v208 offset:12288\n"
        "v_mfma_f32_32x32x16_bf16 v[96:111], v[184:187], v[200:203], v[96:111]\n"
        "s_add_i32 %[sn], %[sn], 1\n"
        "s_cmp_eq_u32 %[sn], 3\n"
        "s_cselect_b32 %[sn], 0, %[sn]\n"
        "v_mfma_f32_32x32x16_bf16 v[112:127], v[188:191], v[200:203], v[112:127]\n"
        "v_mfma_f32_32x32x16_bf16 v[128:143], v[184:187], v[204:207], v[128:143]\n"
        "s_sub_i32 %[n], %[n], 1\n"
        "v_mfma_f32_32x32x16_bf16 v[144:159], v[188:191], v[204:207], v[144:159]\n"
        "s_cmp_lg_u32 %[n], 0\n"
        "s_cbranch_scc1 Lloop%=\n"
        "s_waitcnt lgkmcnt(0)\n"
        "s_nop 15\n"
        "s_nop 3\n"
        : "+{v[32:47]}"(acc[0][0]), "+{v[48:63]}"(acc[0][1]), "+{v[64:79]}"(acc[1][0]), "+{v[80:95]}"(acc[1][1]), "+{v[96:111]}"(acc[2][0]), "+{v[112:127]}"(acc[2][1]), "+{v[128:143]}"(acc[3][0]), "+{v[144:159]}"(acc[3][1]), [n] "+s"(n), [sn] "+s"(sn), [dl] "=&s"(dl)
        : [xa0] "v"(xa[0]), [xa1] "v"(xa[1]), [xa2] "v"(xa[2]), [xa3] "v"(xa[3]), [wa0] "v"(wa[0]), [wa1] "v"(wa[1]), [dm] "s"(-98304), [dp] "s"(49152)
        : "memory", "scc", "v160", "v161", "v162", "v163", "v164", "v165", "v166", "v167", "v168", "v169", "v170", "v171", "v172", "v173", "v174", "v175", "v176", "v177", "v178", "v179", "v180", "v181", "v182", "v183", "v184", "v185", "v186", "v187", "v188", "v189", "v190", "v191", "v192", "v193", "v194", "v195", "v196", "v197", "v198", "v199", "v200", "v201", "v202", "v203", "v204", "v205", "v206", "v207", "v208", "v209", "v210", "v211", "v212", "v213");
}
DI void kb_128x64_nn6k2(f32x16 (&acc)[2][2], const unsigned (&xa)[2], const unsigned (&wa)[2], int n, int sn) {
    unsigned dl;
    asm volatile(
        "s_nop 4\n"
        "v_mov_b32 v128, %[xa0]\n"
        "v_mov_b32 v129, %[xa1]\n"
        "v_mov_b32 v130, %[wa0]\n"
        "v_mov_b32 v131, %[wa1]\n"
        "ds_read_b64_tr_b16 v[96:97], v130 offset:0\n"
        "ds_read_b64_tr_b16 v[98:99], v130 offset:512\n"
        "ds_read_b128 v[104:107], v128 offset:0\n"
        "ds_read_b64_tr_b16 v[100:101], v131 offset:0\n"
        "ds_read_b64_tr_b16 v[102:103], v131 offset:512\n"
        "ds_read_b128 v[108:111], v128 offset:4096\n"
        "Lloop%=:\n"
        "s_waitcnt lgkmcnt(0)\n"
        "v_mfma_f32_32x32x16_bf16 v[32:47], v[96:99], v[104:107], v[32:47]\n"
        "ds_read_b64_tr_b16 v[112:113], v130 offset:2048\n"
        "ds_read_b64_tr_b16 v[114:115], v130 offset:2560\n"
        "s_cmp_eq_u32 %[sn], 0\n"
        "s_cselect_b32 %[dl], %[dm], %[dp]\n"
        "v_mfma_f32_32x32x16_bf16 v[48:63], v[100:103], v[104:107], v[48:63]\n"
        "ds_read_b128 v[120:123], v129 offset:0\n"
        "ds_read_b64_tr_b16 v[116:117], v131 offset:2048\n"
        "v_mfma_f32_32x32x16_bf16 v[64:79], v[96:99], v[108:111], v[64:79]\n"
        "ds_read_b64_tr_b16 v[118:119], v131 offset:2560\n"
        "ds_read_b128 v[124:127], v129 offset:4096\n"
        "v_mfma_f32_32x32x16_bf16 v[80:95], v[100:103], v[108:111], v[80:95]\n"
        "v_add_u32 v128, %[dl], v128\n"
        "v_add_u32 v130, %[dl], v130\n"
        "v_add_u32 v131, %[dl], v131\n"
        "s_waitcnt lgkmcnt(0)\n"
        "s_barrier\n"
        "v_mfma_f32_32x32x16_bf16 v[32:47], v[112:115], v[120:123], v[32:47]\n"
        "ds_read_b64_tr_b16 v[96:97], v130 offset:0\n"
        "ds_read_b64_tr_b16 v[98:99], v130 offset:512\n"
        "v_add_u32 v129, %[dl], v129\n"
        "s_add_i32 %[sn], %[sn], 1\n"
        "s_cmp_eq_u32 %[sn], 6\n"
        "s_cselect_b32 %[sn], 0, %[sn]\n"
        "v_mfma_f32_32x32x16_bf16 v[48:63], v[116:119], v[120:123], v[48:63]\n"
        "ds_read_b128 v[104:107], v128 offset:0\n"
        "ds_read_b64_tr_b16 v[100:101], v131 offset:0\n"
        "v_mfma_f32_32x32x16_bf16 v[64:79], v[112:115], v[124:127], v[64:79]\n"
        "ds_read_b64_tr_b16 v[102:103], v131 offset:512\n"
        "ds_read_b128 v[108:111], v128 offset:4096\n"
        "s_sub_i32 %[n], %[n], 1\n"
        "v_mfma_f32_32x32x16_bf16 v[80:95], v[116:119], v[124:127], v[80:95]\n"
        "s_cmp_lg_u32 %[n], 0\n"
        "s_cbranch_scc1 Lloop%=\n"
        "s_waitcnt lgkmcnt(0)\n"
        "s_nop 15\n"
        "s_nop 3\n"
        : "+{v[32:47]}"(acc[0][0]), "+{v[48:63]}"(acc[0][1]), "+{v[64:79]}"(acc[1][0]), "+{v[80:95]}"(acc[1][1]), [n] "+s"(n), [sn] "+s"(sn), [dl] "=&s"(dl)
        : [xa0] "v"(xa[0]), [xa1] "v"(xa[1]), [wa0] "v"(wa[0]), [wa1] "v"(wa[1]), [dm] "s"(-122880), [dp] "s"(24576)
        : "memory", "scc", "v96", "v97", "v98", "v99", "v100", "v101", "v102", "v103", "v104", "v105", "v106", "v107", "v108", "v109", "v110", "v111", "v112", "v113", "v114", "v115", "v116", "v117", "v118", "v119", "v120", "v121", "v122", "v123", "v124", "v125", "v126", "v127", "v128", "v129", "v130", "v131");
}

template <int BM, int BN, int WTM, int WTN, int KG, int S, bool NN>
struct Gemm5 {
    static constexpr int NWM = BM / WTM, NWN = BN / WTN, KS = 4 / KG;
    static_assert(NWM * NWN * KG == 4, "4 consumers");
    static constexpr int XB = BM * 128, WB = BN * 128, SB = XB + WB;
    static constexpr int TM = WTM / 32, TN = WTN / 32;
    static constexpr int RING_BYTES = S * SB;
    static constexpr int LAG = (TM * TN >= 8 || KS <= 2) ? 0 : 1;
    static constexpr int NJX = BM / 8, NJW = BN / 8, NJ = NJX + NJW, NI = NJ / 4;
    static_assert(NJ % 4 == 0 && (S - 1) * NI <= 63, "");
    struct Prod { unsigned vx[4], vw[4]; long wstep, xg, wg; const char *x1, *w1, *x2, *w2; int seg; unsigned ring; };
    static DI void prod_init(Prod& P, const u16* Xp, int ldx, const u16* Wp, int ldw, const u16* X2p, const u16* W2p, int seg, unsigned ring) {
        const int lane = threadIdx.x & 63;
        constexpr int RPI = NN ? 512 / BN : 8;
#pragma unroll
        for (int i = 0; i < 4; ++i) {
            { const int row = 8 * i + (lane >> 3); const int ch = (lane & 7) ^ ((row >> 1) & 7); P.vx[i] = (unsigned)(row * ldx * 2 + ch * 16 - i * 1024 + 4096); }
            if (!NN) { const int row = 8 * i + (lane >> 3); const int ch = (lane & 7) ^ ((row >> 1) & 7); P.vw[i] = (unsigned)(row * ldw * 2 + ch * 16 - i * 1024 + 4096); }
            else if (BN == 128) { const int k = 4 * i + (lane >> 4); const int ch = (lane & 15) ^ ((k & 3) << 2); P.vw[i] = (unsigned)(k * ldw * 2 + ch * 16 - i * 1024 + 4096); }
            else if (BN == 64) { const int k = 8 * i + (lane >> 3); const int ch = (lane & 7) ^ (((k >> 1) & 1) << 2); P.vw[i] = (unsigned)(k * ldw * 2 + ch * 16 - i * 1024 + 4096); }
        }
        P.wstep = NN ? (long)64 * ldw * 2 : 128; P.xg = (long)32 * ldx * 2; P.wg = (long)4 * RPI * ldw * 2;
        P.x1 = (const char*)Xp - 4096; P.w1 = (const char*)Wp - 4096; P.x2 = (const char*)X2p - 4096; P.w2 = (const char*)W2p - 4096; P.seg = seg; P.ring = ring;
    }
    template <int PW> static DI void iss_t(const Prod& P, int kt, int slot) {
        const bool s2 = kt >= P.seg; const int kk = s2 ? kt - P.seg : kt;
        const unsigned sb = P.ring + slot * SB;
        const char* xb = (s2 ? P.x2 : P.x1) + (long)kk * 128; const char* wb = (s2 ? P.w2 : P.w1) + (long)kk * P.wstep;
#pragma unroll
        for (int j = PW * NI; j < (PW + 1) * NI; ++j) {
            const bool isx = j < NJX; const int jj = isx ? j : j - NJX, g = jj >> 2, i = jj & 3;
            if (i == 0 || j == PW * NI) set_m0(sb + (isx ? 0 : XB) + g * 4096);
            const char* p = isx ? xb + g * P.xg : wb + g * P.wg; const unsigned vo = isx ? P.vx[i] : P.vw[i];
            if (i == 0) glds16<0>(vo, p); else if (i == 1) glds16<1024>(vo, p); else if (i == 2) glds16<2048>(vo, p); else glds16<3072>(vo, p);
        }
    }
    static DI void iss(const Prod& P, int pw, int kt, int slot) {
        if (pw == 0) iss_t<0>(P, kt, slot); else if (pw == 1) iss_t<1>(P, kt, slot); else if (pw == 2) iss_t<2>(P, kt, slot); else iss_t<3>(P, kt, slot);
    }
    static DI void prod_run(const Prod& P, int pw, int nkt) { prod_first(P, pw); prod_rest(P, pw, nkt); }
    static DI void prod_first(const Prod& P, int pw) {
        iss(P, pw, 0, 0); iss(P, pw, 1, 1);
        wait_vm<NI>(); __builtin_amdgcn_s_barrier();
    }
    static DI void prod_rest(const Prod& P, int pw, int nkt) {
#pragma unroll
        for (int s = 2; s < S; ++s) iss(P, pw, s, s);
        int slot = 0;
        for (int j = 1; j < nkt; ++j) {
            const int issued = (j - 2 - LAG + S < S - 1) ? S - 1 : (j - 2 - LAG + S < nkt - 1 ? j - 2 - LAG + S : nkt - 1);
            const int allow = issued - j;
            if (allow >= 4) wait_vm<(4 * NI <= 63 ? 4 * NI : 63)>(); else if (allow == 3) wait_vm<(3 * NI <= 63 ? 3 * NI : 63)>(); else if (allow == 2) wait_vm<2 * NI>(); else if (allow == 1) wait_vm<NI>(); else wait_vm<0>();
            __builtin_amdgcn_s_barrier();
            const int nk = j - 1 - LAG + S;
            if (j - 1 - LAG >= 0) { if (nk < nkt) iss(P, pw, nk, slot); ++slot; if (slot == S) slot = 0; }
        }
        __builtin_amdgcn_s_barrier();
    }
    template <class KL> static DI void consume(f32x16 (&acc)[TM][TN], int wid, int nkt, unsigned ring, KL kloop) {
        const int lane = threadIdx.x & 63;
        const int kg = wid / (NWM * NWN), wmn = wid % (NWM * NWN), wm = wmn / NWN, wn = wmn % NWN;
        const int r32 = lane & 31, h = lane >> 5;
        unsigned xa[KS], wa[NN ? TN : KS];
        { const int rx = wm * WTM + r32; const unsigned a0 = rx * 128 + ((h ^ ((rx >> 1) & 7)) << 4);
#pragma unroll
          for (int i = 0; i < KS; ++i) xa[i] = ring + (a0 ^ ((kg * KS + i) << 5));
          if constexpr (!NN) { const int rw = wn * WTN + pi32(r32); const unsigned b0 = XB + rw * 128 + ((h ^ ((rw >> 1) & 7)) << 4);
#pragma unroll
              for (int i = 0; i < KS; ++i) wa[i] = ring + (b0 ^ ((kg * KS + i) << 5)); } }
        if constexpr (NN) { const int g1 = (lane >> 4) & 1, p = lane & 3, q = (lane & 15) >> 2; const int cb = (p & 1) * 16 + g1 * 8 + (p >> 1) * 4;
            const int swz = (BN == 64) ? ((q >> 1) & 1) : q;
#pragma unroll
            for (int tn = 0; tn < TN; ++tn) { const int c = wn * TN + tn; wa[tn] = ring + XB + (8 * h + q + kg * KS * 16) * (BN * 2) + ((c ^ swz) * 64) + cb * 2; } }
#pragma unroll
        for (int i = 0; i < TM; ++i)
#pragma unroll
            for (int j = 0; j < TN; ++j) acc[i][j] = f32x16{};
        asm volatile("" ::: "memory"); __builtin_amdgcn_s_barrier(); asm volatile("" ::: "memory");
        kloop(acc, xa, wa, nkt, 1 % S);
    }
    static constexpr int ROWB = BN * 2 + 16, STG_BYTES = BM * ROWB;
    static DI void epilogue(const f32x16 (&acc)[TM][TN], int wid, char* stg, u16* out, long ldo, float scale) {
        const int lane = threadIdx.x & 63;
        lds_bar();
        if (KG == 1) {
            if (wid < 4) {
                const int wm = wid / NWN, wn = wid % NWN;
#pragma unroll
                for (int i = 0; i < TM; ++i)
#pragma unroll
                    for (int j = 0; j < TN; ++j) {
                        char* d = stg + (wm * WTM + i * 32 + (lane & 31)) * ROWB + (wn * WTN + j * 32 + (lane >> 5) * 16) * 2; const f32x16 a = acc[i][j] * scale;
                        *(u32x4*)(d) = (u32x4){cvtpk(a[0], a[1]), cvtpk(a[2], a[3]), cvtpk(a[4], a[5]), cvtpk(a[6], a[7])};
                        *(u32x4*)(d + 16) = (u32x4){cvtpk(a[8], a[9]), cvtpk(a[10], a[11]), cvtpk(a[12], a[13]), cvtpk(a[14], a[15])};
                    }
            }
        } else {
            static_assert(KG <= 2, "");
            const int kg = wid / (NWM * NWN), wmn = wid % (NWM * NWN), wm = wmn / NWN, wn = wmn % NWN;
            char* part = stg + STG_BYTES;
            if (wid < 4 && kg == 1) {
#pragma unroll
                for (int i = 0; i < TM; ++i)
#pragma unroll
                    for (int j = 0; j < TN; ++j)
#pragma unroll
                        for (int r = 0; r < 4; ++r) *(f32x4*)(part + ((wmn * TM * TN + i * TN + j) * 4 + r) * 1024 + lane * 16) = (f32x4){acc[i][j][4 * r], acc[i][j][4 * r + 1], acc[i][j][4 * r + 2], acc[i][j][4 * r + 3]};
            }
            lds_bar();
            if (wid < 4 && kg == 0) {
#pragma unroll
                for (int i = 0; i < TM; ++i)
#pragma unroll
                    for (int j = 0; j < TN; ++j) {
                        f32x16 a = acc[i][j];
#pragma unroll
                        for (int r = 0; r < 4; ++r) { const f32x4 v = *(const f32x4*)(part + ((wmn * TM * TN + i * TN + j) * 4 + r) * 1024 + lane * 16);
                            a[4 * r] += v[0]; a[4 * r + 1] += v[1]; a[4 * r + 2] += v[2]; a[4 * r + 3] += v[3]; }
                        a = a * scale;
                        char* d = stg + (wm * WTM + i * 32 + (lane & 31)) * ROWB + (wn * WTN + j * 32 + (lane >> 5) * 16) * 2;
                        *(u32x4*)(d) = (u32x4){cvtpk(a[0], a[1]), cvtpk(a[2], a[3]), cvtpk(a[4], a[5]), cvtpk(a[6], a[7])};
                        *(u32x4*)(d + 16) = (u32x4){cvtpk(a[8], a[9]), cvtpk(a[10], a[11]), cvtpk(a[12], a[13]), cvtpk(a[14], a[15])};
                    }
            }
        }
        lds_bar();
        constexpr int LPR = BN / 8, RPI = 64 / LPR, NIT = BM / (8 * RPI);
#pragma unroll
        for (int it = 0; it < NIT; ++it) {
            const int row = (it * 8 + wid) * RPI + lane / LPR, c = lane % LPR;
            ST16(out + (long)row * ldo + c * 8, *(const u32x4*)(stg + row * ROWB + c * 16));
        }
    }
};
DI void fused_loop(f32x16& OA, f32x16& OB, f32x4& LA, f32x4& LB, f32x16 (&acc)[2][2], const f32x16& NA, const f32x16& NB, const bf16x8& qa0, const bf16x8& qa1, const bf16x8& qb0, const bf16x8& qb1, const bf16x8& ones,
        unsigned a0, unsigned (&xa)[4], unsigned (&wa)[4]) {
    int n = 8;
    asm volatile(
        "s_nop 4\n"
        "ds_read_b128 v[210:213], v250 offset:0\n"
        "ds_read_b128 v[214:217], v250 offset:1024\n"
        "ds_read_b128 v[226:229], v246 offset:0\n"
        "ds_read_b128 v[234:237], v242 offset:0\n"
        "ds_read_b128 v[230:233], v246 offset:4096\n"
        "ds_read_b128 v[238:241], v242 offset:4096\n"
        "v_mov_b32 v202, 0\n"
        "v_mov_b32 v203, 0\n"
        "v_mov_b32 v204, 0\n"
        "v_mov_b32 v205, 0\n"
        "v_mov_b32 v206, 0\n"
        "v_mov_b32 v207, 0\n"
        "v_mov_b32 v208, 0\n"
        "v_mov_b32 v209, 0\n"
        "v_mov_b32 v218, 0\n"
        "v_mov_b32 v219, 0\n"
        "v_mov_b32 v220, 0\n"
        "v_mov_b32 v221, 0\n"
        "v_mov_b32 v222, 0\n"
        "v_mov_b32 v223, 0\n"
        "v_mov_b32 v224, 0\n"
        "v_mov_b32 v225, 0\n"
        "s_waitcnt lgkmcnt(0)\n"
        "v_mfma_f32_32x32x16_bf16 v[70:85], v[210:213], v[174:177], v[134:149]\n"
        "v_mfma_f32_32x32x16_bf16 v[70:85], v[214:217], v[178:181], v[70:85]\n"
        "s_nop 7\n"
        "s_nop 7\n"
        "Lloop%=:\n"
        "v_mfma_f32_32x32x16_bf16 v[86:101], v[210:213], v[182:185], v[150:165]\n"
        "v_exp_f32 v70, v70\n"
        "v_exp_f32 v71, v71\n"
        "s_waitcnt lgkmcnt(0)\n"
        "v_mfma_f32_32x32x16_bf16 v[6:21], v[226:229], v[234:237], v[6:21]\n"
        "v_exp_f32 v72, v72\n"
        "v_exp_f32 v73, v73\n"
        "v_cvt_pk_bf16_f32 v194, v70, v71\n"
        "v_mfma_f32_32x32x16_bf16 v[22:37], v[230:233], v[234:237], v[22:37]\n"
        "ds_read_b128 v[234:237], v243 offset:0\n"
        "v_exp_f32 v74, v74\n"
        "v_cvt_pk_bf16_f32 v195, v72, v73\n"
        "v_exp_f32 v75, v75\n"
        "v_mfma_f32_32x32x16_bf16 v[86:101], v[214:217], v[186:189], v[86:101]\n"
        "ds_read_b128 v[210:213], v250 offset:4096\n"
        "ds_read_b128 v[214:217], v250 offset:5120\n"
        "v_exp_f32 v76, v76\n"
        "v_cvt_pk_bf16_f32 v196, v74, v75\n"
        "v_exp_f32 v77, v77\n"
        "v_mfma_f32_32x32x16_bf16 v[38:53], v[226:229], v[238:241], v[38:53]\n"
        "ds_read_b128 v[226:229], v247 offset:0\n"
        "v_exp_f32 v78, v78\n"
        "v_cvt_pk_bf16_f32 v197, v76, v77\n"
        "v_exp_f32 v79, v79\n"
        "v_mfma_f32_32x32x16_bf16 v[54:69], v[230:233], v[238:241], v[54:69]\n"
        "ds_read_b128 v[230:233], v247 offset:4096\n"
        "ds_read_b128 v[238:241], v243 offset:4096\n"
        "v_exp_f32 v80, v80\n"
        "v_cvt_pk_bf16_f32 v198, v78, v79\n"
        "v_mfma_f32_32x32x16_bf16 v[118:133], v[218:221], v[202:205], v[118:133]\n"
        "v_exp_f32 v81, v81\n"
        "v_exp_f32 v82, v82\n"
        "v_cvt_pk_bf16_f32 v199, v80, v81\n"
        "v_mfma_f32_32x32x16_bf16 v[118:133], v[222:225], v[206:209], v[118:133]\n"
        "ds_read_b128 v[218:221], v250 offset:2048\n"
        "ds_read_b128 v[222:225], v250 offset:3072\n"
        "v_exp_f32 v83, v83\n"
        "v_exp_f32 v84, v84\n"
        "v_cvt_pk_bf16_f32 v200, v82, v83\n"
        "v_mfma_f32_16x16x32_bf16 v[170:173], v[190:193], v[202:205], v[170:173]\n"
        "v_exp_f32 v85, v85\n"
        "v_mfma_f32_16x16x32_bf16 v[170:173], v[190:193], v[206:209], v[170:173]\n"
        "s_nop 0\n"
        "v_cvt_pk_bf16_f32 v201, v84, v85\n"
        "s_waitcnt lgkmcnt(5)\n"
        "v_mfma_f32_32x32x16_bf16 v[70:85], v[210:213], v[174:177], v[134:149]\n"
        "v_exp_f32 v86, v86\n"
        "v_exp_f32 v87, v87\n"
        "s_waitcnt lgkmcnt(2)\n"
        "v_mfma_f32_32x32x16_bf16 v[6:21], v[226:229], v[234:237], v[6:21]\n"
        "v_exp_f32 v88, v88\n"
        "v_exp_f32 v89, v89\n"
        "v_cvt_pk_bf16_f32 v202, v86, v87\n"
        "v_mfma_f32_32x32x16_bf16 v[22:37], v[230:233], v[234:237], v[22:37]\n"
        "ds_read_b128 v[234:237], v244 offset:0\n"
        "v_exp_f32 v90, v90\n"
        "v_cvt_pk_bf16_f32 v203, v88, v89\n"
        "v_exp_f32 v91, v91\n"
        "v_mfma_f32_32x32x16_bf16 v[70:85], v[214:217], v[178:181], v[70:85]\n"
        "v_exp_f32 v92, v92\n"
        "v_cvt_pk_bf16_f32 v204, v90, v91\n"
        "v_exp_f32 v93, v93\n"
        "v_mfma_f32_32x32x16_bf16 v[38:53], v[226:229], v[238:241], v[38:53]\n"
        "ds_read_b128 v[226:229], v248 offset:0\n"
        "v_exp_f32 v94, v94\n"
        "v_cvt_pk_bf16_f32 v205, v92, v93\n"
        "v_exp_f32 v95, v95\n"
        "v_mfma_f32_32x32x16_bf16 v[54:69], v[230:233], v[238:241], v[54:69]\n"
        "ds_read_b128 v[230:233], v248 offset:4096\n"
        "ds_read_b128 v[238:241], v244 offset:4096\n"
        "v_exp_f32 v96, v96\n"
        "v_cvt_pk_bf16_f32 v206, v94, v95\n"
        "s_waitcnt lgkmcnt(5)\n"
        "v_mfma_f32_32x32x16_bf16 v[102:117], v[218:221], v[194:197], v[102:117]\n"
        "v_exp_f32 v97, v97\n"
        "v_exp_f32 v98, v98\n"
        "v_cvt_pk_bf16_f32 v207, v96, v97\n"
        "s_waitcnt lgkmcnt(4)\n"
        "v_mfma_f32_32x32x16_bf16 v[102:117], v[222:225], v[198:201], v[102:117]\n"
        "v_exp_f32 v99, v99\n"
        "v_exp_f32 v100, v100\n"
        "v_cvt_pk_bf16_f32 v208, v98, v99\n"
        "v_mfma_f32_16x16x32_bf16 v[166:169], v[190:193], v[194:197], v[166:169]\n"
        "v_exp_f32 v101, v101\n"
        "v_mfma_f32_16x16x32_bf16 v[166:169], v[190:193], v[198:201], v[166:169]\n"
        "s_nop 0\n"
        "v_cvt_pk_bf16_f32 v209, v100, v101\n"
        "s_barrier\n"
        "v_mfma_f32_32x32x16_bf16 v[86:101], v[210:213], v[182:185], v[150:165]\n"
        "v_exp_f32 v70, v70\n"
        "v_exp_f32 v71, v71\n"
        "s_waitcnt lgkmcnt(0)\n"
        "v_mfma_f32_32x32x16_bf16 v[6:21], v[226:229], v[234:237], v[6:21]\n"
        "v_exp_f32 v72, v72\n"
        "v_exp_f32 v73, v73\n"
        "v_cvt_pk_bf16_f32 v194, v70, v71\n"
        "v_mfma_f32_32x32x16_bf16 v[22:37], v[230:233], v[234:237], v[22:37]\n"
        "ds_read_b128 v[234:237], v245 offset:0\n"
        "v_exp_f32 v74, v74\n"
        "v_cvt_pk_bf16_f32 v195, v72, v73\n"
        "v_exp_f32 v75, v75\n"
        "v_mfma_f32_32x32x16_bf16 v[86:101], v[214:217], v[186:189], v[86:101]\n"
        "ds_read_b128 v[210:213], v250 offset:8192\n"
        "ds_read_b128 v[214:217], v250 offset:9216\n"
        "v_exp_f32 v76, v76\n"
        "v_cvt_pk_bf16_f32 v196, v74, v75\n"
        "v_exp_f32 v77, v77\n"
        "v_mfma_f32_32x32x16_bf16 v[38:53], v[226:229], v[238:241], v[38:53]\n"
        "ds_read_b128 v[226:229], v249 offset:0\n"
        "v_exp_f32 v78, v78\n"
        "v_cvt_pk_bf16_f32 v197, v76, v77\n"
        "v_exp_f32 v79, v79\n"
        "v_mfma_f32_32x32x16_bf16 v[54:69], v[230:233], v[238:241], v[54:69]\n"
        "ds_read_b128 v[230:233], v249 offset:4096\n"
        "ds_read_b128 v[238:241], v245 offset:4096\n"
        "v_exp_f32 v80, v80\n"
        "v_cvt_pk_bf16_f32 v198, v78, v79\n"
        "v_mfma_f32_32x32x16_bf16 v[118:133], v[218:221], v[202:205], v[118:133]\n"
        "v_exp_f32 v81, v81\n"
        "v_exp_f32 v82, v82\n"
        "v_cvt_pk_bf16_f32 v199, v80, v81\n"
        "v_mfma_f32_32x32x16_bf16 v[118:133], v[222:225], v[206:209], v[118:133]\n"
        "ds_read_b128 v[218:221], v250 offset:6144\n"
        "ds_read_b128 v[222:225], v250 offset:7168\n"
        "v_exp_f32 v83, v83\n"
        "v_exp_f32 v84, v84\n"
        "v_cvt_pk_bf16_f32 v200, v82, v83\n"
        "v_mfma_f32_16x16x32_bf16 v[170:173], v[190:193], v[202:205], v[170:173]\n"
        "v_exp_f32 v85, v85\n"
        "v_mfma_f32_16x16x32_bf16 v[170:173], v[190:193], v[206:209], v[170:173]\n"
        "s_nop 0\n"
        "v_cvt_pk_bf16_f32 v201, v84, v85\n"
        "s_waitcnt lgkmcnt(5)\n"
        "v_mfma_f32_32x32x16_bf16 v[70:85], v[210:213], v[174:177], v[134:149]\n"
        "v_exp_f32 v86, v86\n"
        "v_exp_f32 v87, v87\n"
        "s_waitcnt lgkmcnt(2)\n"
        "v_mfma_f32_32x32x16_bf16 v[6:21], v[226:229], v[234:237], v[6:21]\n"
        "v_exp_f32 v88, v88\n"
        "v_exp_f32 v89, v89\n"
        "v_cvt_pk_bf16_f32 v202, v86, v87\n"
        "v_mfma_f32_32x32x16_bf16 v[22:37], v[230:233], v[234:237], v[22:37]\n"
        "ds_read_b128 v[234:237], v242 offset:32768\n"
        "v_exp_f32 v90, v90\n"
        "v_cvt_pk_bf16_f32 v203, v88, v89\n"
        "v_exp_f32 v91, v91\n"
        "v_mfma_f32_32x32x16_bf16 v[70:85], v[214:217], v[178:181], v[70:85]\n"
        "v_exp_f32 v92, v92\n"
        "v_cvt_pk_bf16_f32 v204, v90, v91\n"
        "v_exp_f32 v93, v93\n"
        "v_mfma_f32_32x32x16_bf16 v[38:53], v[226:229], v[238:241], v[38:53]\n"
        "ds_read_b128 v[226:229], v246 offset:32768\n"
        "v_exp_f32 v94, v94\n"
        "v_cvt_pk_bf16_f32 v205, v92, v93\n"
        "v_exp_f32 v95, v95\n"
        "v_mfma_f32_32x32x16_bf16 v[54:69], v[230:233], v[238:241], v[54:69]\n"
        "ds_read_b128 v[230:233], v246 offset:36864\n"
        "ds_read_b128 v[238:241], v242 offset:36864\n"
        "v_exp_f32 v96, v96\n"
        "v_cvt_pk_bf16_f32 v206, v94, v95\n"
        "s_waitcnt lgkmcnt(5)\n"
        "v_mfma_f32_32x32x16_bf16 v[102:117], v[218:221], v[194:197], v[102:117]\n"
        "v_exp_f32 v97, v97\n"
        "v_exp_f32 v98, v98\n"
        "v_cvt_pk_bf16_f32 v207, v96, v97\n"
        "s_waitcnt lgkmcnt(4)\n"
        "v_mfma_f32_32x32x16_bf16 v[102:117], v[222:225], v[198:201], v[102:117]\n"
        "v_exp_f32 v99, v99\n"
        "v_exp_f32 v100, v100\n"
        "v_cvt_pk_bf16_f32 v208, v98, v99\n"
        "v_mfma_f32_16x16x32_bf16 v[166:169], v[190:193], v[194:197], v[166:169]\n"
        "v_exp_f32 v101, v101\n"
        "v_mfma_f32_16x16x32_bf16 v[166:169], v[190:193], v[198:201], v[166:169]\n"
        "s_nop 0\n"
        "v_cvt_pk_bf16_f32 v209, v100, v101\n"
        "v_mfma_f32_32x32x16_bf16 v[86:101], v[210:213], v[182:185], v[150:165]\n"
        "v_exp_f32 v70, v70\n"
        "v_exp_f32 v71, v71\n"
        "s_waitcnt lgkmcnt(0)\n"
        "v_mfma_f32_32x32x16_bf16 v[6:21], v[226:229], v[234:237], v[6:21]\n"
        "v_exp_f32 v72, v72\n"
        "v_exp_f32 v73, v73\n"
        "v_cvt_pk_bf16_f32 v194, v70, v71\n"
        "v_mfma_f32_32x32x16_bf16 v[22:37], v[230:233], v[234:237], v[22:37]\n"
        "ds_read_b128 v[234:237], v243 offset:32768\n"
        "v_exp_f32 v74, v74\n"
        "v_cvt_pk_bf16_f32 v195, v72, v73\n"
        "v_exp_f32 v75, v75\n"
        "v_mfma_f32_32x32x16_bf16 v[86:101], v[214:217], v[186:189], v[86:101]\n"
        "ds_read_b128 v[210:213], v250 offset:12288\n"
        "ds_read_b128 v[214:217], v250 offset:13312\n"
        "v_exp_f32 v76, v76\n"
        "v_cvt_pk_bf16_f32 v196, v74, v75\n"
        "v_exp_f32 v77, v77\n"
        "v_mfma_f32_32x32x16_bf16 v[38:53], v[226:229], v[238:241], v[38:53]\n"
        "ds_read_b128 v[226:229], v247 offset:32768\n"
        "v_exp_f32 v78, v78\n"
        "v_cvt_pk_bf16_f32 v197, v76, v77\n"
        "v_exp_f32 v79, v79\n"
        "v_mfma_f32_32x32x16_bf16 v[54:69], v[230:233], v[238:241], v[54:69]\n"
        "ds_read_b128 v[230:233], v247 offset:36864\n"
        "ds_read_b128 v[238:241], v243 offset:36864\n"
        "v_exp_f32 v80, v80\n"
        "v_cvt_pk_bf16_f32 v198, v78, v79\n"
        "v_mfma_f32_32x32x16_bf16 v[118:133], v[218:221], v[202:205], v[118:133]\n"
        "v_exp_f32 v81, v81\n"
        "v_exp_f32 v82, v82\n"
        "v_cvt_pk_bf16_f32 v199, v80, v81\n"
        "v_mfma_f32_32x32x16_bf16 v[118:133], v[222:225], v[206:209], v[118:133]\n"
        "ds_read_b128 v[218:221], v250 offset:10240\n"
        "ds_read_b128 v[222:225], v250 offset:11264\n"
        "v_exp_f32 v83, v83\n"
        "v_exp_f32 v84, v84\n"
        "v_cvt_pk_bf16_f32 v200, v82, v83\n"
        "v_mfma_f32_16x16x32_bf16 v[170:173], v[190:193], v[202:205], v[170:173]\n"
        "v_exp_f32 v85, v85\n"
        "v_mfma_f32_16x16x32_bf16 v[170:173], v[190:193], v[206:209], v[170:173]\n"
        "s_nop 0\n"
        "v_cvt_pk_bf16_f32 v201, v84, v85\n"
        "s_waitcnt lgkmcnt(5)\n"
        "v_mfma_f32_32x32x16_bf16 v[70:85], v[210:213], v[174:177], v[134:149]\n"
        "v_exp_f32 v86, v86\n"
        "v_exp_f32 v87, v87\n"
        "s_waitcnt lgkmcnt(2)\n"
        "v_mfma_f32_32x32x16_bf16 v[6:21], v[226:229], v[234:237], v[6:21]\n"
        "v_exp_f32 v88, v88\n"
        "v_exp_f32 v89, v89\n"
        "v_cvt_pk_bf16_f32 v202, v86, v87\n"
        "v_mfma_f32_32x32x16_bf16 v[22:37], v[230:233], v[234:237], v[22:37]\n"
        "ds_read_b128 v[234:237], v244 offset:32768\n"
        "v_exp_f32 v90, v90\n"
        "v_cvt_pk_bf16_f32 v203, v88, v89\n"
        "v_exp_f32 v91, v91\n"
        "v_mfma_f32_32x32x16_bf16 v[70:85], v[214:217], v[178:181], v[70:85]\n"
        "v_exp_f32 v92, v92\n"
        "v_cvt_pk_bf16_f32 v204, v90, v91\n"
        "v_exp_f32 v93, v93\n"
        "v_mfma_f32_32x32x16_bf16 v[38:53], v[226:229], v[238:241], v[38:53]\n"
        "ds_read_b128 v[226:229], v248 offset:32768\n"
        "v_exp_f32 v94, v94\n"
        "v_cvt_pk_bf16_f32 v205, v92, v93\n"
        "v_exp_f32 v95, v95\n"
        "v_mfma_f32_32x32x16_bf16 v[54:69], v[230:233], v[238:241], v[54:69]\n"
        "ds_read_b128 v[230:233], v248 offset:36864\n"
        "ds_read_b128 v[238:241], v244 offset:36864\n"
        "v_exp_f32 v96, v96\n"
        "v_cvt_pk_bf16_f32 v206, v94, v95\n"
        "s_waitcnt lgkmcnt(5)\n"
        "v_mfma_f32_32x32x16_bf16 v[102:117], v[218:221], v[194:197], v[102:117]\n"
        "v_exp_f32 v97, v97\n"
        "v_exp_f32 v98, v98\n"
        "v_cvt_pk_bf16_f32 v207, v96, v97\n"
        "v_add_u32 v242, 0x10000, v242\n"
        "s_waitcnt lgkmcnt(4)\n"
        "v_mfma_f32_32x32x16_bf16 v[102:117], v[222:225], v[198:201], v[102:117]\n"
        "v_exp_f32 v99, v99\n"
        "v_exp_f32 v100, v100\n"
        "v_cvt_pk_bf16_f32 v208, v98, v99\n"
        "v_add_u32 v246, 0x10000, v246\n"
        "v_mfma_f32_16x16x32_bf16 v[166:169], v[190:193], v[194:197], v[166:169]\n"
        "v_exp_f32 v101, v101\n"
        "v_mfma_f32_16x16x32_bf16 v[166:169], v[190:193], v[198:201], v[166:169]\n"
        "s_nop 0\n"
        "v_cvt_pk_bf16_f32 v209, v100, v101\n"
        "s_barrier\n"
        "v_mfma_f32_32x32x16_bf16 v[86:101], v[210:213], v[182:185], v[150:165]\n"
        "v_exp_f32 v70, v70\n"
        "v_exp_f32 v71, v71\n"
        "s_waitcnt lgkmcnt(0)\n"
        "v_mfma_f32_32x32x16_bf16 v[6:21], v[226:229], v[234:237], v[6:21]\n"
        "v_exp_f32 v72, v72\n"
        "v_exp_f32 v73, v73\n"
        "v_cvt_pk_bf16_f32 v194, v70, v71\n"
        "v_mfma_f32_32x32x16_bf16 v[22:37], v[230:233], v[234:237], v[22:37]\n"
        "ds_read_b128 v[234:237], v245 offset:32768\n"
        "v_exp_f32 v74, v74\n"
        "v_cvt_pk_bf16_f32 v195, v72, v73\n"
        "v_exp_f32 v75, v75\n"
        "v_mfma_f32_32x32x16_bf16 v[86:101], v[214:217], v[186:189], v[86:101]\n"
        "ds_read_b128 v[210:213], v250 offset:16384\n"
        "ds_read_b128 v[214:217], v250 offset:17408\n"
        "v_exp_f32 v76, v76\n"
        "v_cvt_pk_bf16_f32 v196, v74, v75\n"
        "v_exp_f32 v77, v77\n"
        "v_mfma_f32_32x32x16_bf16 v[38:53], v[226:229], v[238:241], v[38:53]\n"
        "ds_read_b128 v[226:229], v249 offset:32768\n"
        "v_exp_f32 v78, v78\n"
        "v_cvt_pk_bf16_f32 v197, v76, v77\n"
        "v_exp_f32 v79, v79\n"
        "v_mfma_f32_32x32x16_bf16 v[54:69], v[230:233], v[238:241], v[54:69]\n"
        "ds_read_b128 v[230:233], v249 offset:36864\n"
        "ds_read_b128 v[238:241], v245 offset:36864\n"
        "v_exp_f32 v80, v80\n"
        "v_cvt_pk_bf16_f32 v198, v78, v79\n"
        "v_mfma_f32_32x32x16_bf16 v[118:133], v[218:221], v[202:205], v[118:133]\n"
        "v_exp_f32 v81, v81\n"
        "v_exp_f32 v82, v82\n"
        "v_cvt_pk_bf16_f32 v199, v80, v81\n"
        "v_add_u32 v243, 0x10000, v243\n"
        "v_mfma_f32_32x32x16_bf16 v[118:133], v[222:225], v[206:209], v[118:133]\n"
        "ds_read_b128 v[218:221], v250 offset:14336\n"
        "ds_read_b128 v[222:225], v250 offset:15360\n"
        "v_exp_f32 v83, v83\n"
        "v_exp_f32 v84, v84\n"
        "v_cvt_pk_bf16_f32 v200, v82, v83\n"
        "v_add_u32 v247, 0x10000, v247\n"
        "v_mfma_f32_16x16x32_bf16 v[170:173], v[190:193], v[202:205], v[170:173]\n"
        "v_exp_f32 v85, v85\n"
        "v_mfma_f32_16x16x32_bf16 v[170:173], v[190:193], v[206:209], v[170:173]\n"
        "s_nop 0\n"
        "v_cvt_pk_bf16_f32 v201, v84, v85\n"
        "s_waitcnt lgkmcnt(5)\n"
        "v_mfma_f32_32x32x16_bf16 v[70:85], v[210:213], v[174:177], v[134:149]\n"
        "v_exp_f32 v86, v86\n"
        "v_exp_f32 v87, v87\n"
        "s_waitcnt lgkmcnt(2)\n"
        "v_mfma_f32_32x32x16_bf16 v[6:21], v[226:229], v[234:237], v[6:21]\n"
        "v_exp_f32 v88, v88\n"
        "v_exp_f32 v89, v89\n"
        "v_cvt_pk_bf16_f32 v202, v86, v87\n"
        "v_mfma_f32_32x32x16_bf16 v[22:37], v[230:233], v[234:237], v[22:37]\n"
        "ds_read_b128 v[234:237], v242 offset:0\n"
        "v_exp_f32 v90, v90\n"
        "v_cvt_pk_bf16_f32 v203, v88, v89\n"
        "v_exp_f32 v91, v91\n"
        "v_mfma_f32_32x32x16_bf16 v[70:85], v[214:217], v[178:181], v[70:85]\n"
        "v_exp_f32 v92, v92\n"
        "v_cvt_pk_bf16_f32 v204, v90, v91\n"
        "v_exp_f32 v93, v93\n"
        "v_mfma_f32_32x32x16_bf16 v[38:53], v[226:229], v[238:241], v[38:53]\n"
        "ds_read_b128 v[226:229], v246 offset:0\n"
        "v_exp_f32 v94, v94\n"
        "v_cvt_pk_bf16_f32 v205, v92, v93\n"
        "v_exp_f32 v95, v95\n"
        "v_mfma_f32_32x32x16_bf16 v[54:69], v[230:233], v[238:241], v[54:69]\n"
        "ds_read_b128 v[230:233], v246 offset:4096\n"
        "ds_read_b128 v[238:241], v242 offset:4096\n"
        "v_exp_f32 v96, v96\n"
        "v_cvt_pk_bf16_f32 v206, v94, v95\n"
        "s_waitcnt lgkmcnt(5)\n"
        "v_mfma_f32_32x32x16_bf16 v[102:117], v[218:221], v[194:197], v[102:117]\n"
        "v_exp_f32 v97, v97\n"
        "v_exp_f32 v98, v98\n"
        "v_cvt_pk_bf16_f32 v207, v96, v97\n"
        "v_add_u32 v244, 0x10000, v244\n"
        "s_waitcnt lgkmcnt(4)\n"
        "v_mfma_f32_32x32x16_bf16 v[102:117], v[222:225], v[198:201], v[102:117]\n"
        "v_exp_f32 v99, v99\n"
        "v_exp_f32 v100, v100\n"
        "v_cvt_pk_bf16_f32 v208, v98, v99\n"
        "v_add_u32 v248, 0x10000, v248\n"
        "v_mfma_f32_16x16x32_bf16 v[166:169], v[190:193], v[194:197], v[166:169]\n"
        "v_exp_f32 v101, v101\n"
        "v_add_u32 v245, 0x10000, v245\n"
        "v_mfma_f32_16x16x32_bf16 v[166:169], v[190:193], v[198:201], v[166:169]\n"
        "v_add_u32 v249, 0x10000, v249\n"
        "v_cvt_pk_bf16_f32 v209, v100, v101\n"
        "v_mfma_f32_32x32x16_bf16 v[86:101], v[210:213], v[182:185], v[150:165]\n"
        "v_exp_f32 v70, v70\n"
        "v_exp_f32 v71, v71\n"
        "s_waitcnt lgkmcnt(0)\n"
        "v_mfma_f32_32x32x16_bf16 v[6:21], v[226:229], v[234:237], v[6:21]\n"
        "v_exp_f32 v72, v72\n"
        "v_exp_f32 v73, v73\n"
        "v_cvt_pk_bf16_f32 v194, v70, v71\n"
        "v_mfma_f32_32x32x16_bf16 v[22:37], v[230:233], v[234:237], v[22:37]\n"
        "ds_read_b128 v[234:237], v243 offset:0\n"
        "v_exp_f32 v74, v74\n"
        "v_cvt_pk_bf16_f32 v195, v72, v73\n"
        "v_exp_f32 v75, v75\n"
        "v_mfma_f32_32x32x16_bf16 v[86:101], v[214:217], v[186:189], v[86:101]\n"
        "ds_read_b128 v[210:213], v250 offset:20480\n"
        "ds_read_b128 v[214:217], v250 offset:21504\n"
        "v_exp_f32 v76, v76\n"
        "v_cvt_pk_bf16_f32 v196, v74, v75\n"
        "v_exp_f32 v77, v77\n"
        "v_mfma_f32_32x32x16_bf16 v[38:53], v[226:229], v[238:241], v[38:53]\n"
        "ds_read_b128 v[226:229], v247 offset:0\n"
        "v_exp_f32 v78, v78\n"
        "v_cvt_pk_bf16_f32 v197, v76, v77\n"
        "v_exp_f32 v79, v79\n"
        "v_mfma_f32_32x32x16_bf16 v[54:69], v[230:233], v[238:241], v[54:69]\n"
        "ds_read_b128 v[230:233], v247 offset:4096\n"
        "ds_read_b128 v[238:241], v243 offset:4096\n"
        "v_exp_f32 v80, v80\n"
        "v_cvt_pk_bf16_f32 v198, v78, v79\n"
        "v_mfma_f32_32x32x16_bf16 v[118:133], v[218:221], v[202:205], v[118:133]\n"
        "v_exp_f32 v81, v81\n"
        "v_exp_f32 v82, v82\n"
        "v_cvt_pk_bf16_f32 v199, v80, v81\n"
        "v_mfma_f32_32x32x16_bf16 v[118:133], v[222:225], v[206:209], v[118:133]\n"
        "ds_read_b128 v[218:221], v250 offset:18432\n"
        "ds_read_b128 v[222:225], v250 offset:19456\n"
        "v_exp_f32 v83, v83\n"
        "v_exp_f32 v84, v84\n"
        "v_cvt_pk_bf16_f32 v200, v82, v83\n"
        "v_mfma_f32_16x16x32_bf16 v[170:173], v[190:193], v[202:205], v[170:173]\n"
        "v_exp_f32 v85, v85\n"
        "v_mfma_f32_16x16x32_bf16 v[170:173], v[190:193], v[206:209], v[170:173]\n"
        "s_nop 0\n"
        "v_cvt_pk_bf16_f32 v201, v84, v85\n"
        "s_waitcnt lgkmcnt(5)\n"
        "v_mfma_f32_32x32x16_bf16 v[70:85], v[210:213], v[174:177], v[134:149]\n"
        "v_exp_f32 v86, v86\n"
        "v_exp_f32 v87, v87\n"
        "s_waitcnt lgkmcnt(2)\n"
        "v_mfma_f32_32x32x16_bf16 v[6:21], v[226:229], v[234:237], v[6:21]\n"
        "v_exp_f32 v88, v88\n"
        "v_exp_f32 v89, v89\n"
        "v_cvt_pk_bf16_f32 v202, v86, v87\n"
        "v_mfma_f32_32x32x16_bf16 v[22:37], v[230:233], v[234:237], v[22:37]\n"
        "ds_read_b128 v[234:237], v244 offset:0\n"
        "v_exp_f32 v90, v90\n"
        "v_cvt_pk_bf16_f32 v203, v88, v89\n"
        "v_exp_f32 v91, v91\n"
        "v_mfma_f32_32x32x16_bf16 v[70:85], v[214:217], v[178:181], v[70:85]\n"
        "v_exp_f32 v92, v92\n"
        "v_cvt_pk_bf16_f32 v204, v90, v91\n"
        "v_exp_f32 v93, v93\n"
        "v_mfma_f32_32x32x16_bf16 v[38:53], v[226:229], v[238:241], v[38:53]\n"
        "ds_read_b128 v[226:229], v248 offset:0\n"
        "v_exp_f32 v94, v94\n"
        "v_cvt_pk_bf16_f32 v205, v92, v93\n"
        "v_exp_f32 v95, v95\n"
        "v_mfma_f32_32x32x16_bf16 v[54:69], v[230:233], v[238:241], v[54:69]\n"
        "ds_read_b128 v[230:233], v248 offset:4096\n"
        "ds_read_b128 v[238:241], v244 offset:4096\n"
        "v_exp_f32 v96, v96\n"
        "v_cvt_pk_bf16_f32 v206, v94, v95\n"
        "s_waitcnt lgkmcnt(5)\n"
        "v_mfma_f32_32x32x16_bf16 v[102:117], v[218:221], v[194:197], v[102:117]\n"
        "v_exp_f32 v97, v97\n"
        "v_exp_f32 v98, v98\n"
        "v_cvt_pk_bf16_f32 v207, v96, v97\n"
        "s_waitcnt lgkmcnt(4)\n"
        "v_mfma_f32_32x32x16_bf16 v[102:117], v[222:225], v[198:201], v[102:117]\n"
        "v_exp_f32 v99, v99\n"
        "v_exp_f32 v100, v100\n"
        "v_cvt_pk_bf16_f32 v208, v98, v99\n"
        "v_mfma_f32_16x16x32_bf16 v[166:169], v[190:193], v[194:197], v[166:169]\n"
        "v_exp_f32 v101, v101\n"
        "v_mfma_f32_16x16x32_bf16 v[166:169], v[190:193], v[198:201], v[166:169]\n"
        "s_nop 0\n"
        "v_cvt_pk_bf16_f32 v209, v100, v101\n"
        "s_barrier\n"
        "v_mfma_f32_32x32x16_bf16 v[86:101], v[210:213], v[182:185], v[150:165]\n"
        "v_exp_f32 v70, v70\n"
        "v_exp_f32 v71, v71\n"
        "s_waitcnt lgkmcnt(0)\n"
        "v_mfma_f32_32x32x16_bf16 v[6:21], v[226:229], v[234:237], v[6:21]\n"
        "v_exp_f32 v72, v72\n"
        "v_exp_f32 v73, v73\n"
        "v_cvt_pk_bf16_f32 v194, v70, v71\n"
        "v_mfma_f32_32x32x16_bf16 v[22:37], v[230:233], v[234:237], v[22:37]\n"
        "ds_read_b128 v[234:237], v245 offset:0\n"
        "v_exp_f32 v74, v74\n"
        "v_cvt_pk_bf16_f32 v195, v72, v73\n"
        "v_exp_f32 v75, v75\n"
        "v_mfma_f32_32x32x16_bf16 v[86:101], v[214:217], v[186:189], v[86:101]\n"
        "ds_read_b128 v[210:213], v250 offset:24576\n"
        "ds_read_b128 v[214:217], v250 offset:25600\n"
        "v_exp_f32 v76, v76\n"
        "v_cvt_pk_bf16_f32 v196, v74, v75\n"
        "v_exp_f32 v77, v77\n"
        "v_mfma_f32_32x32x16_bf16 v[38:53], v[226:229], v[238:241], v[38:53]\n"
        "ds_read_b128 v[226:229], v249 offset:0\n"
        "v_exp_f32 v78, v78\n"
        "v_cvt_pk_bf16_f32 v197, v76, v77\n"
        "v_exp_f32 v79, v79\n"
        "v_mfma_f32_32x32x16_bf16 v[54:69], v[230:233], v[238:241], v[54:69]\n"
        "ds_read_b128 v[230:233], v249 offset:4096\n"
        "ds_read_b128 v[238:241], v245 offset:4096\n"
        "v_exp_f32 v80, v80\n"
        "v_cvt_pk_bf16_f32 v198, v78, v79\n"
        "v_mfma_f32_32x32x16_bf16 v[118:133], v[218:221], v[202:205], v[118:133]\n"
        "v_exp_f32 v81, v81\n"
        "v_exp_f32 v82, v82\n"
        "v_cvt_pk_bf16_f32 v199, v80, v81\n"
        "v_mfma_f32_32x32x16_bf16 v[118:133], v[222:225], v[206:209], v[118:133]\n"
        "ds_read_b128 v[218:221], v250 offset:22528\n"
        "ds_read_b128 v[222:225], v250 offset:23552\n"
        "v_exp_f32 v83, v83\n"
        "v_exp_f32 v84, v84\n"
        "v_cvt_pk_bf16_f32 v200, v82, v83\n"
        "v_mfma_f32_16x16x32_bf16 v[170:173], v[190:193], v[202:205], v[170:173]\n"
        "v_exp_f32 v85, v85\n"
        "v_mfma_f32_16x16x32_bf16 v[170:173], v[190:193], v[206:209], v[170:173]\n"
        "s_nop 0\n"
        "v_cvt_pk_bf16_f32 v201, v84, v85\n"
        "s_waitcnt lgkmcnt(5)\n"
        "v_mfma_f32_32x32x16_bf16 v[70:85], v[210:213], v[174:177], v[134:149]\n"
        "v_exp_f32 v86, v86\n"
        "v_exp_f32 v87, v87\n"
        "s_waitcnt lgkmcnt(2)\n"
        "v_mfma_f32_32x32x16_bf16 v[6:21], v[226:229], v[234:237], v[6:21]\n"
        "v_exp_f32 v88, v88\n"
        "v_exp_f32 v89, v89\n"
        "v_cvt_pk_bf16_f32 v202, v86, v87\n"
        "v_mfma_f32_32x32x16_bf16 v[22:37], v[230:233], v[234:237], v[22:37]\n"
        "ds_read_b128 v[234:237], v242 offset:32768\n"
        "v_exp_f32 v90, v90\n"
        "v_cvt_pk_bf16_f32 v203, v88, v89\n"
        "v_exp_f32 v91, v91\n"
        "v_mfma_f32_32x32x16_bf16 v[70:85], v[214:217], v[178:181], v[70:85]\n"
        "v_exp_f32 v92, v92\n"
        "v_cvt_pk_bf16_f32 v204, v90, v91\n"
        "v_exp_f32 v93, v93\n"
        "v_mfma_f32_32x32x16_bf16 v[38:53], v[226:229], v[238:241], v[38:53]\n"
        "ds_read_b128 v[226:229], v246 offset:32768\n"
        "v_exp_f32 v94, v94\n"
        "v_cvt_pk_bf16_f32 v205, v92, v93\n"
        "v_exp_f32 v95, v95\n"
        "v_mfma_f32_32x32x16_bf16 v[54:69], v[230:233], v[238:241], v[54:69]\n"
        "ds_read_b128 v[230:233], v246 offset:36864\n"
        "ds_read_b128 v[238:241], v242 offset:36864\n"
        "v_exp_f32 v96, v96\n"
        "v_cvt_pk_bf16_f32 v206, v94, v95\n"
        "s_waitcnt lgkmcnt(5)\n"
        "v_mfma_f32_32x32x16_bf16 v[102:117], v[218:221], v[194:197], v[102:117]\n"
        "v_exp_f32 v97, v97\n"
        "v_exp_f32 v98, v98\n"
        "v_cvt_pk_bf16_f32 v207, v96, v97\n"
        "s_waitcnt lgkmcnt(4)\n"
        "v_mfma_f32_32x32x16_bf16 v[102:117], v[222:225], v[198:201], v[102:117]\n"
        "v_exp_f32 v99, v99\n"
        "v_exp_f32 v100, v100\n"
        "v_cvt_pk_bf16_f32 v208, v98, v99\n"
        "v_mfma_f32_16x16x32_bf16 v[166:169], v[190:193], v[194:197], v[166:169]\n"
        "v_exp_f32 v101, v101\n"
        "v_mfma_f32_16x16x32_bf16 v[166:169], v[190:193], v[198:201], v[166:169]\n"
        "s_nop 0\n"
        "v_cvt_pk_bf16_f32 v209, v100, v101\n"
        "v_mfma_f32_32x32x16_bf16 v[86:101], v[210:213], v[182:185], v[150:165]\n"
        "v_exp_f32 v70, v70\n"
        "v_exp_f32 v71, v71\n"
        "s_waitcnt lgkmcnt(0)\n"
        "v_mfma_f32_32x32x16_bf16 v[6:21], v[226:229], v[234:237], v[6:21]\n"
        "v_exp_f32 v72, v72\n"
        "v_exp_f32 v73, v73\n"
        "v_cvt_pk_bf16_f32 v194, v70, v71\n"
        "v_mfma_f32_32x32x16_bf16 v[22:37], v[230:233], v[234:237], v[22:37]\n"
        "ds_read_b128 v[234:237], v243 offset:32768\n"
        "v_exp_f32 v74, v74\n"
        "v_cvt_pk_bf16_f32 v195, v72, v73\n"
        "v_exp_f32 v75, v75\n"
        "v_mfma_f32_32x32x16_bf16 v[86:101], v[214:217], v[186:189], v[86:101]\n"
        "ds_read_b128 v[210:213], v250 offset:28672\n"
        "ds_read_b128 v[214:217], v250 offset:29696\n"
        "v_exp_f32 v76, v76\n"
        "v_cvt_pk_bf16_f32 v196, v74, v75\n"
        "v_exp_f32 v77, v77\n"
        "v_mfma_f32_32x32x16_bf16 v[38:53], v[226:229], v[238:241], v[38:53]\n"
        "ds_read_b128 v[226:229], v247 offset:32768\n"
        "v_exp_f32 v78, v78\n"
        "v_cvt_pk_bf16_f32 v197, v76, v77\n"
        "v_exp_f32 v79, v79\n"
        "v_mfma_f32_32x32x16_bf16 v[54:69], v[230:233], v[238:241], v[54:69]\n"
        "ds_read_b128 v[230:233], v247 offset:36864\n"
        "ds_read_b128 v[238:241], v243 offset:36864\n"
        "v_exp_f32 v80, v80\n"
        "v_cvt_pk_bf16_f32 v198, v78, v79\n"
        "v_mfma_f32_32x32x16_bf16 v[118:133], v[218:221], v[202:205], v[118:133]\n"
        "v_exp_f32 v81, v81\n"
        "v_exp_f32 v82, v82\n"
        "v_cvt_pk_bf16_f32 v199, v80, v81\n"
        "v_mfma_f32_32x32x16_bf16 v[118:133], v[222:225], v[206:209], v[118:133]\n"
        "ds_read_b128 v[218:221], v250 offset:26624\n"
        "ds_read_b128 v[222:225], v250 offset:27648\n"
        "v_exp_f32 v83, v83\n"
        "v_exp_f32 v84, v84\n"
        "v_cvt_pk_bf16_f32 v200, v82, v83\n"
        "v_mfma_f32_16x16x32_bf16 v[170:173], v[190:193], v[202:205], v[170:173]\n"
        "v_exp_f32 v85, v85\n"
        "v_mfma_f32_16x16x32_bf16 v[170:173], v[190:193], v[206:209], v[170:173]\n"
        "s_nop 0\n"
        "v_cvt_pk_bf16_f32 v201, v84, v85\n"
        "s_waitcnt lgkmcnt(5)\n"
        "v_mfma_f32_32x32x16_bf16 v[70:85], v[210:213], v[174:177], v[134:149]\n"
        "v_exp_f32 v86, v86\n"
        "v_exp_f32 v87, v87\n"
        "s_waitcnt lgkmcnt(2)\n"
        "v_mfma_f32_32x32x16_bf16 v[6:21], v[226:229], v[234:237], v[6:21]\n"
        "v_exp_f32 v88, v88\n"
        "v_exp_f32 v89, v89\n"
        "v_cvt_pk_bf16_f32 v202, v86, v87\n"
        "v_mfma_f32_32x32x16_bf16 v[22:37], v[230:233], v[234:237], v[22:37]\n"
        "ds_read_b128 v[234:237], v244 offset:32768\n"
        "v_exp_f32 v90, v90\n"
        "v_cvt_pk_bf16_f32 v203, v88, v89\n"
        "v_exp_f32 v91, v91\n"
        "v_mfma_f32_32x32x16_bf16 v[70:85], v[214:217], v[178:181], v[70:85]\n"
        "v_exp_f32 v92, v92\n"
        "v_cvt_pk_bf16_f32 v204, v90, v91\n"
        "v_exp_f32 v93, v93\n"
        "v_mfma_f32_32x32x16_bf16 v[38:53], v[226:229], v[238:241], v[38:53]\n"
        "ds_read_b128 v[226:229], v248 offset:32768\n"
        "v_exp_f32 v94, v94\n"
        "v_cvt_pk_bf16_f32 v205, v92, v93\n"
        "v_exp_f32 v95, v95\n"
        "v_mfma_f32_32x32x16_bf16 v[54:69], v[230:233], v[238:241], v[54:69]\n"
        "ds_read_b128 v[230:233], v248 offset:36864\n"
        "ds_read_b128 v[238:241], v244 offset:36864\n"
        "v_exp_f32 v96, v96\n"
        "v_cvt_pk_bf16_f32 v206, v94, v95\n"
        "s_waitcnt lgkmcnt(5)\n"
        "v_mfma_f32_32x32x16_bf16 v[102:117], v[218:221], v[194:197], v[102:117]\n"
        "v_exp_f32 v97, v97\n"
        "v_exp_f32 v98, v98\n"
        "v_cvt_pk_bf16_f32 v207, v96, v97\n"
        "v_add_u32 v242, 0xffff0000, v242\n"
        "s_waitcnt lgkmcnt(4)\n"
        "v_mfma_f32_32x32x16_bf16 v[102:117], v[222:225], v[198:201], v[102:117]\n"
        "v_exp_f32 v99, v99\n"
        "v_exp_f32 v100, v100\n"
        "v_cvt_pk_bf16_f32 v208, v98, v99\n"
        "v_add_u32 v246, 0xffff0000, v246\n"
        "v_mfma_f32_16x16x32_bf16 v[166:169], v[190:193], v[194:197], v[166:169]\n"
        "v_exp_f32 v101, v101\n"
        "v_mfma_f32_16x16x32_bf16 v[166:169], v[190:193], v[198:201], v[166:169]\n"
        "s_nop 0\n"
        "v_cvt_pk_bf16_f32 v209, v100, v101\n"
        "s_barrier\n"
        "v_mfma_f32_32x32x16_bf16 v[86:101], v[210:213], v[182:185], v[150:165]\n"
        "v_exp_f32 v70, v70\n"
        "v_exp_f32 v71, v71\n"
        "s_waitcnt lgkmcnt(0)\n"
        "v_mfma_f32_32x32x16_bf16 v[6:21], v[226:229], v[234:237], v[6:21]\n"
        "v_exp_f32 v72, v72\n"
        "v_exp_f32 v73, v73\n"
        "v_cvt_pk_bf16_f32 v194, v70, v71\n"
        "v_mfma_f32_32x32x16_bf16 v[22:37], v[230:233], v[234:237], v[22:37]\n"
        "ds_read_b128 v[234:237], v245 offset:32768\n"
        "v_exp_f32 v74, v74\n"
        "v_cvt_pk_bf16_f32 v195, v72, v73\n"
        "v_exp_f32 v75, v75\n"
        "v_mfma_f32_32x32x16_bf16 v[86:101], v[214:217], v[186:189], v[86:101]\n"
        "ds_read_b128 v[210:213], v250 offset:0\n"
        "ds_read_b128 v[214:217], v250 offset:1024\n"
        "v_exp_f32 v76, v76\n"
        "v_cvt_pk_bf16_f32 v196, v74, v75\n"
        "v_exp_f32 v77, v77\n"
        "v_mfma_f32_32x32x16_bf16 v[38:53], v[226:229], v[238:241], v[38:53]\n"
        "ds_read_b128 v[226:229], v249 offset:32768\n"
        "v_exp_f32 v78, v78\n"
        "v_cvt_pk_bf16_f32 v197, v76, v77\n"
        "v_exp_f32 v79, v79\n"
        "v_mfma_f32_32x32x16_bf16 v[54:69], v[230:233], v[238:241], v[54:69]\n"
        "ds_read_b128 v[230:233], v249 offset:36864\n"
        "ds_read_b128 v[238:241], v245 offset:36864\n"
        "v_exp_f32 v80, v80\n"
        "v_cvt_pk_bf16_f32 v198, v78, v79\n"
        "v_mfma_f32_32x32x16_bf16 v[118:133], v[218:221], v[202:205], v[118:133]\n"
        "v_exp_f32 v81, v81\n"
        "v_exp_f32 v82, v82\n"
        "v_cvt_pk_bf16_f32 v199, v80, v81\n"
        "v_add_u32 v243, 0xffff0000, v243\n"
        "v_mfma_f32_32x32x16_bf16 v[118:133], v[222:225], v[206:209], v[118:133]\n"
        "ds_read_b128 v[218:221], v250 offset:30720\n"
        "ds_read_b128 v[222:225], v250 offset:31744\n"
        "v_exp_f32 v83, v83\n"
        "v_exp_f32 v84, v84\n"
        "v_cvt_pk_bf16_f32 v200, v82, v83\n"
        "v_add_u32 v247, 0xffff0000, v247\n"
        "v_mfma_f32_16x16x32_bf16 v[170:173], v[190:193], v[202:205], v[170:173]\n"
        "v_exp_f32 v85, v85\n"
        "v_mfma_f32_16x16x32_bf16 v[170:173], v[190:193], v[206:209], v[170:173]\n"
        "s_nop 0\n"
        "v_cvt_pk_bf16_f32 v201, v84, v85\n"
        "s_waitcnt lgkmcnt(5)\n"
        "v_mfma_f32_32x32x16_bf16 v[70:85], v[210:213], v[174:177], v[134:149]\n"
        "v_exp_f32 v86, v86\n"
        "v_exp_f32 v87, v87\n"
        "s_waitcnt lgkmcnt(2)\n"
        "v_mfma_f32_32x32x16_bf16 v[6:21], v[226:229], v[234:237], v[6:21]\n"
        "v_exp_f32 v88, v88\n"
        "v_exp_f32 v89, v89\n"
        "v_cvt_pk_bf16_f32 v202, v86, v87\n"
        "v_mfma_f32_32x32x16_bf16 v[22:37], v[230:233], v[234:237], v[22:37]\n"
        "ds_read_b128 v[234:237], v242 offset:0\n"
        "v_exp_f32 v90, v90\n"
        "v_cvt_pk_bf16_f32 v203, v88, v89\n"
        "v_exp_f32 v91, v91\n"
        "v_mfma_f32_32x32x16_bf16 v[70:85], v[214:217], v[178:181], v[70:85]\n"
        "v_exp_f32 v92, v92\n"
        "v_cvt_pk_bf16_f32 v204, v90, v91\n"
        "v_exp_f32 v93, v93\n"
        "v_mfma_f32_32x32x16_bf16 v[38:53], v[226:229], v[238:241], v[38:53]\n"
        "ds_read_b128 v[226:229], v246 offset:0\n"
        "v_exp_f32 v94, v94\n"
        "v_cvt_pk_bf16_f32 v205, v92, v93\n"
        "v_exp_f32 v95, v95\n"
        "v_mfma_f32_32x32x16_bf16 v[54:69], v[230:233], v[238:241], v[54:69]\n"
        "ds_read_b128 v[230:233], v246 offset:4096\n"
        "ds_read_b128 v[238:241], v242 offset:4096\n"
        "v_exp_f32 v96, v96\n"
        "v_cvt_pk_bf16_f32 v206, v94, v95\n"
        "s_waitcnt lgkmcnt(5)\n"
        "v_mfma_f32_32x32x16_bf16 v[102:117], v[218:221], v[194:197], v[102:117]\n"
        "v_exp_f32 v97, v97\n"
        "v_exp_f32 v98, v98\n"
        "v_cvt_pk_bf16_f32 v207, v96, v97\n"
        "v_add_u32 v244, 0xffff0000, v244\n"
        "s_waitcnt lgkmcnt(4)\n"
        "v_mfma_f32_32x32x16_bf16 v[102:117], v[222:225], v[198:201], v[102:117]\n"
        "v_exp_f32 v99, v99\n"
        "v_exp_f32 v100, v100\n"
        "v_cvt_pk_bf16_f32 v208, v98, v99\n"
        "v_add_u32 v248, 0xffff0000, v248\n"
        "v_mfma_f32_16x16x32_bf16 v[166:169], v[190:193], v[194:197], v[166:169]\n"
        "v_exp_f32 v101, v101\n"
        "v_add_u32 v245, 0xffff0000, v245\n"
        "v_mfma_f32_16x16x32_bf16 v[166:169], v[190:193], v[198:201], v[166:169]\n"
        "v_add_u32 v249, 0xffff0000, v249\n"
        "v_cvt_pk_bf16_f32 v209, v100, v101\n"
        "s_sub_i32 %[n], %[n], 1\n"
        "s_cmp_lg_u32 %[n], 0\n"
        "s_cbranch_scc1 Lloop%=\n"
        "v_mfma_f32_32x32x16_bf16 v[118:133], v[218:221], v[202:205], v[118:133]\n"
        "v_mfma_f32_32x32x16_bf16 v[118:133], v[222:225], v[206:209], v[118:133]\n"
        "v_mfma_f32_16x16x32_bf16 v[170:173], v[190:193], v[202:205], v[170:173]\n"
        "v_mfma_f32_16x16x32_bf16 v[170:173], v[190:193], v[206:209], v[170:173]\n"
        "s_waitcnt lgkmcnt(0)\n"
        "s_nop 15\n"
        "s_nop 7\n"
        : "+{v[102:117]}"(OA), "+{v[118:133]}"(OB), "+{v[166:169]}"(LA), "+{v[170:173]}"(LB), "+{v[6:21]}"(acc[0][0]), "+{v[22:37]}"(acc[0][1]), "+{v[38:53]}"(acc[1][0]), "+{v[54:69]}"(acc[1][1]), "+{v242}"(xa[0]), "+{v243}"(xa[1]), "+{v244}"(xa[2]), "+{v245}"(xa[3]), "+{v246}"(wa[0]), "+{v247}"(wa[1]), "+{v248}"(wa[2]), "+{v249}"(wa[3]), [n] "+s"(n)
        : "{v[134:149]}"(NA), "{v[150:165]}"(NB), "{v[174:177]}"(qa0), "{v[178:181]}"(qa1), "{v[182:185]}"(qb0), "{v[186:189]}"(qb1), "{v[190:193]}"(ones), "{v250}"(a0)
        : "memory", "scc", "v70", "v71", "v72", "v73", "v74", "v75", "v76", "v77", "v78", "v79", "v80", "v81", "v82", "v83", "v84", "v85", "v86", "v87", "v88", "v89", "v90", "v91", "v92", "v93", "v94", "v95", "v96", "v97", "v98", "v99", "v100", "v101", "v194", "v195", "v196", "v197", "v198", "v199", "v200", "v201", "v202", "v203", "v204", "v205", "v206", "v207", "v208", "v209", "v210", "v211", "v212", "v213", "v214", "v215", "v216", "v217", "v218", "v219", "v220", "v221", "v222", "v223", "v224", "v225", "v226", "v227", "v228", "v229", "v230", "v231", "v232", "v233", "v234", "v235", "v236", "v237", "v238", "v239", "v240", "v241");
}

constexpr float ATT_MARGIN = 85.f;
constexpr int KV_RING = 4 * 8192, Y_RING = 4 * 32768, FUSED_LDS = KV_RING + Y_RING;
struct PK { u32x4 a, b; };
using GYP = Gemm5<128, 128, 64, 64, 1, 4, false>;
DI void fused_produce(int pw, const char* __restrict__ KV, const u16* Xp, const u16* Wp, unsigned lds0) {
    const unsigned voff = (threadIdx.x & 63) * 16;
    auto kv = [&](int stage) {
        const char* g = KV + (long)stage * 8192 + pw * 2048;
        set_m0(lds0 + (stage & 3) * 8192 + pw * 2048);
        glds16<0>(voff, g); glds16<1024>(voff, g);
    };
    GYP::Prod P;
    auto st = [&](int stage) { kv(stage); GYP::iss(P, pw, stage, stage & 3); };
    kv(0);
    GYP::prod_init(P, Xp, 2048, Wp, 2048, Xp, Wp, 32, lds0 + KV_RING);
    GYP::iss(P, pw, 0, 0); st(1);
    wait_vm<18>(); __builtin_amdgcn_s_barrier();
    wait_vm<10>(); __builtin_amdgcn_s_barrier();
    st(2); st(3);
    wait_vm<20>(); __builtin_amdgcn_s_barrier();
    for (int j = 2; j < 31; ++j) {
        wait_vm<10>(); __builtin_amdgcn_s_barrier();
        if (j + 2 < 32) st(j + 2);
    }
    wait_vm<0>(); __builtin_amdgcn_s_barrier();
    __builtin_amdgcn_s_barrier();
}
DI void fused_finish(f32x16& OA, f32x16& OB, const f32x4& LA, const f32x4& LB, const u16* __restrict__ Q, const char* __restrict__ KV, u16* __restrict__ O, int p0, int h0) {
    int tz = threadIdx.x; asm volatile("" : "+v"(tz));
    const int lane = tz & 63, r32 = lane & 31, h = lane >> 5;
    const u16* qrow = Q + (long)(p0 + r32) * 1024 + h0 * 32 + h * 8;
    float la, lbv;
    { const float a0 = __shfl(LA[0], lane & 15), a1 = __shfl(LA[1], lane & 15), b0 = __shfl(LB[0], lane & 15), b1 = __shfl(LB[1], lane & 15);
      la = (lane & 16) ? a1 : a0; lbv = (lane & 16) ? b1 : b0; }
    float amax = 0.f, bmax = 0.f; bool bad = !(la < 1e38f) || !(lbv < 1e38f);
#pragma unroll
    for (int i = 0; i < 16; ++i) { bad |= !(fabsf(OA[i]) < 1e38f) | !(fabsf(OB[i]) < 1e38f); amax = fmaxf(amax, fabsf(OA[i])); bmax = fmaxf(bmax, fabsf(OB[i])); }
    bad |= (amax < 1e-30f) | (bmax < 1e-30f);
    if (__builtin_expect(__any(bad), 0)) {
#define BF(x) __builtin_bit_cast(bf16x8, x)
        const bf16x8 ra0 = *(const bf16x8*)(qrow), ra1 = *(const bf16x8*)(qrow + 16), rb0 = *(const bf16x8*)(qrow + 32), rb1 = *(const bf16x8*)(qrow + 48);
        float ma = -INFINITY, mb = -INFINITY; la = 0.f; lbv = 0.f; OA = f32x16{}; OB = f32x16{};
        for (int t = 0; t < 64; ++t) {
            const char* g = KV + (long)t * 4096 + lane * 16;
            const bf16x8 gk0 = *(const bf16x8*)g, gk1 = *(const bf16x8*)(g + 1024), gv0 = *(const bf16x8*)(g + 2048), gv1 = *(const bf16x8*)(g + 3072);
            f32x16 z = {};
            f32x16 a = MFMA32(gk0, ra0, z); a = MFMA32(gk1, ra1, a); f32x16 b = MFMA32(gk0, rb0, z); b = MFMA32(gk1, rb1, b);
            float ta = a[0], tb = b[0];
#pragma unroll
            for (int i = 1; i < 16; ++i) { ta = fmaxf(ta, a[i]); tb = fmaxf(tb, b[i]); }
            ta = fmaxf(ta, __shfl_xor(ta, 32)); tb = fmaxf(tb, __shfl_xor(tb, 32));
            const float na = fmaxf(ma, ta), nb = fmaxf(mb, tb);
            const float fa = __builtin_amdgcn_exp2f(ma - na), fb = __builtin_amdgcn_exp2f(mb - nb); ma = na; mb = nb;
            la *= fa; lbv *= fb;
            PK pa, pb;
#pragma unroll
            for (int i = 0; i < 16; ++i) { OA[i] *= fa; OB[i] *= fb; a[i] = __builtin_amdgcn_exp2f(a[i] - ma); b[i] = __builtin_amdgcn_exp2f(b[i] - mb); }
#pragma unroll
            for (int i = 0; i < 4; ++i) { pa.a[i] = cvtpk(a[2 * i], a[2 * i + 1]); pa.b[i] = cvtpk(a[8 + 2 * i], a[9 + 2 * i]); pb.a[i] = cvtpk(b[2 * i], b[2 * i + 1]); pb.b[i] = cvtpk(b[8 + 2 * i], b[9 + 2 * i]); }
#pragma unroll
            for (int i = 0; i < 4; ++i) { la += __uint_as_float(pa.a[i] << 16) + __uint_as_float(pa.a[i] & 0xffff0000u) + __uint_as_float(pa.b[i] << 16) + __uint_as_float(pa.b[i] & 0xffff0000u);
                                          lbv += __uint_as_float(pb.a[i] << 16) + __uint_as_float(pb.a[i] & 0xffff0000u) + __uint_as_float(pb.b[i] << 16) + __uint_as_float(pb.b[i] & 0xffff0000u); }
            OA = MFMA32(gv0, BF(pa.a), OA); OA = MFMA32(gv1, BF(pa.b), OA); OB = MFMA32(gv0, BF(pb.a), OB); OB = MFMA32(gv1, BF(pb.b), OB);
        }
        la += __shfl_xor(la, 32); lbv += __shfl_xor(lbv, 32);
#undef BF
    }
    const float ia = 1.f / la, ib = 1.f / lbv;
    u16* orow = O + (long)(p0 + r32) * 1024 + h0 * 32 + h * 16;
    ST16(orow, ((u32x4){cvtpk(OA[0] * ia, OA[1] * ia), cvtpk(OA[2] * ia, OA[3] * ia), cvtpk(OA[4] * ia, OA[5] * ia), cvtpk(OA[6] * ia, OA[7] * ia)}));
    ST16(orow + 8, ((u32x4){cvtpk(OA[8] * ia, OA[9] * ia), cvtpk(OA[10] * ia, OA[11] * ia), cvtpk(OA[12] * ia, OA[13] * ia), cvtpk(OA[14] * ia, OA[15] * ia)}));
    ST16(orow + 32, ((u32x4){cvtpk(OB[0] * ib, OB[1] * ib), cvtpk(OB[2] * ib, OB[3] * ib), cvtpk(OB[4] * ib, OB[5] * ib), cvtpk(OB[6] * ib, OB[7] * ib)}));
    ST16(orow + 40, ((u32x4){cvtpk(OB[8] * ib, OB[9] * ib), cvtpk(OB[10] * ib, OB[11] * ib), cvtpk(OB[12] * ib, OB[13] * ib), cvtpk(OB[14] * ib, OB[15] * ib)}));
}

DI void fused_compute(const u16* __restrict__ Q, const char* __restrict__ KV, u16* __restrict__ O, int blk, int wid, char* lds_generic) {
    const int lane = threadIdx.x & 63, r32 = lane & 31, h = lane >> 5;
    lds_cptr lds = (lds_cptr)lds_generic; const unsigned lds0 = (unsigned)(uintptr_t)lds_generic;
    const int p0 = (blk >> 2) * 32, h0 = (blk & 3) * 8 + wid * 2;
    const u16* qrow = Q + (long)(p0 + r32) * 1024 + h0 * 32 + h * 8;
    const bf16x8 qa0 = *(const bf16x8*)(qrow), qa1 = *(const bf16x8*)(qrow + 16), qb0 = *(const bf16x8*)(qrow + 32), qb1 = *(const bf16x8*)(qrow + 48);
    bf16x8 ones; { const short one = (lane == 0 || lane == 32 || lane == 17 || lane == 49) ? (short)0x3F80 : (short)0; ones = (bf16x8){one, one, one, one, one, one, one, one}; }
    unsigned xa[4], wa[4];
    { const int wm = wid >> 1, wn = wid & 1;
      const int rx = wm * 64 + r32; const unsigned a0 = rx * 128 + ((h ^ ((rx >> 1) & 7)) << 4);
      const int rw = wn * 64 + pi32(r32); const unsigned b0 = 16384 + rw * 128 + ((h ^ ((rw >> 1) & 7)) << 4);
#pragma unroll
      for (int i = 0; i < 4; ++i) { xa[i] = lds0 + KV_RING + (a0 ^ (i << 5)); wa[i] = lds0 + KV_RING + (b0 ^ (i << 5)); } }
    f32x16 OA = {}, OB = {}, acc[2][2] = {}; f32x4 LA = {}, LB = {};
    PIN(OA); PIN(OB); PIN(acc[0][0]); PIN(acc[0][1]); PIN(acc[1][0]); PIN(acc[1][1]); PIN(LA); PIN(LB);
    lds_bar();
    f32x16 NA, NB;
    {
        float ma = -INFINITY, mb = -INFINITY;
#pragma unroll
        for (int t = 0; t < 2; ++t) {
            const bf16x8 k0 = *(const LAS bf16x8*)(lds + t * 4096 + lane * 16), k1 = *(const LAS bf16x8*)(lds + t * 4096 + lane * 16 + 1024);
            f32x16 z = {};
            f32x16 a = MFMA32(k0, qa0, z); a = MFMA32(k1, qa1, a); f32x16 b = MFMA32(k0, qb0, z); b = MFMA32(k1, qb1, b);
#pragma unroll
            for (int i = 0; i < 16; ++i) { ma = fmaxf(ma, a[i]); mb = fmaxf(mb, b[i]); }
        }
        ma = fmaxf(ma, __shfl_xor(ma, 32)); mb = fmaxf(mb, __shfl_xor(mb, 32));
#pragma unroll
        for (int i = 0; i < 16; ++i) { NA[i] = -(ma + ATT_MARGIN); NB[i] = -(mb + ATT_MARGIN); }
    }
    PIN(NA); PIN(NB);
    lds_bar();
    fused_loop(OA, OB, LA, LB, acc, NA, NB, qa0, qa1, qb0, qb1, ones, lds0 + lane * 16, xa, wa);
    lds_bar();
    {
        int tz = threadIdx.x; asm volatile("" : "+v"(tz));
        const int ln = tz & 63, wm = wid >> 1, wn = wid & 1;
#pragma unroll
        for (int i = 0; i < 2; ++i)
#pragma unroll
            for (int j = 0; j < 2; ++j) {
                char* d = lds_generic + (wm * 64 + i * 32 + (ln & 31)) * GYP::ROWB + (wn * 64 + j * 32 + (ln >> 5) * 16) * 2; const f32x16 a = acc[i][j];
                *(u32x4*)(d) = (u32x4){cvtpk(a[0], a[1]), cvtpk(a[2], a[3]), cvtpk(a[4], a[5]), cvtpk(a[6], a[7])};
                *(u32x4*)(d + 16) = (u32x4){cvtpk(a[8], a[9]), cvtpk(a[10], a[11]), cvtpk(a[12], a[13]), cvtpk(a[14], a[15])};
            }
    }
    lds_bar();
    fused_finish(OA, OB, LA, LB, Q, KV, O, p0, h0);
}

using GKK = Gemm3<128, 32, 32, 32, 1, 3, true, false>;
using GKV = Gemm3<128, 32, 32, 32, 1, 3, true, true>;
using GAB = Gemm5<256, 128, 128, 64, 1, 3, false>;
using GQ = Gemm5<256, 128, 128, 64, 1, 3, true>;
using GWO = Gemm5<128, 64, 64, 64, 2, 6, true>;
constexpr float QSCALE = 0.17677669529663687f * 1.4426950408889634f;
constexpr int L1 = GAB::RING_BYTES, L2 = FUSED_LDS, L3 = GWO::RING_BYTES;
#define KL(fn) [](auto& acc, const auto& xa, const auto& wa, int n, int sn) { fn(acc, xa, wa, n, sn); }

__global__ __launch_bounds__(512, 2) void k1_proj(const u16* __restrict__ xA, const u16* __restrict__ wq, const u16* __restrict__ wk, const u16* __restrict__ wv, const u16* __restrict__ xB,
                                                  const u16* __restrict__ dre, const u16* __restrict__ dim, u16* __restrict__ Q, char* __restrict__ KV, u16* __restrict__ abT,
                                                  const u16* __restrict__ wo) {
    extern __shared__ __attribute__((aligned(16))) char lds[];
    const int b = blockIdx.x, lane = threadIdx.x & 63; const int wid = __builtin_amdgcn_readfirstlane(threadIdx.x >> 6);
    const unsigned lds0 = (unsigned)(uintptr_t)lds;
    if (b < 128) {
        const int g = b & 7, idx = b >> 3;
        const int m0 = ((g >> 1) * 2 + (idx & 1)) * 256, n0 = ((g & 1) * 8 + (idx >> 1)) * 128;
        const u16* X = m0 < 1024 ? dre + (long)m0 * 1024 : dim + (long)(m0 - 1024) * 1024;
        f32x16 acc[4][2];
        if (wid >= 4) { GAB::Prod P; GAB::prod_init(P, X, 1024, xB + (long)n0 * 1024, 1024, X, xB, 16, lds0); GAB::prod_run(P, wid - 4, 16); }
        else GAB::consume(acc, wid, 16, lds0, KL(kb_256x128_nt3));
        GAB::epilogue(acc, wid, lds, abT + (long)m0 * 2048 + n0, 2048, 1.f);
    } else if (b < 192) {
        const int t = b - 128, g = t & 7, idx = t >> 3;
        const int m0 = ((g >> 1) * 2 + (idx & 1)) * 256, n0 = ((g & 1) * 4 + (idx >> 1)) * 128;
        f32x16 acc[4][2];
        if (wid >= 4) { GQ::Prod P; GQ::prod_init(P, xA + (long)m0 * 1024, 1024, wq + n0, 1024, xA, wq, 16, lds0); GQ::prod_run(P, wid - 4, 16); }
        else GQ::consume(acc, wid, 16, lds0, KL(kb_256x128_nn3));
        GQ::epilogue(acc, wid, lds, Q + (long)m0 * 1024 + n0, 1024, QSCALE);
    } else if (b < 224) {
        if (wid >= 4) return;
        const int bb = b - 192, m0 = (bb >> 1) * 128; const int t = (m0 >> 5) + wid;
        if ((bb & 1) == 0) {
            f32x16 acc[1][1];
            GKK::run(acc, xA + (long)m0 * 1024, 1024, wk, 32, xA, wk, 16, 16, lds);
            char* dst = KV + (long)t * 4096 + (lane >> 5) * 1024 + (lane & 31) * 16; const f32x16 a = acc[0][0];
            ST16(dst, ((u32x4){cvtpk(a[0], a[1]), cvtpk(a[2], a[3]), cvtpk(a[4], a[5]), cvtpk(a[6], a[7])}));
            ST16(dst + 512, ((u32x4){cvtpk(a[8], a[9]), cvtpk(a[10], a[11]), cvtpk(a[12], a[13]), cvtpk(a[14], a[15])}));
        } else {
            f32x16 acc[1][1];
            GKV::run(acc, xA + (long)m0 * 1024, 1024, wv, 32, xA, wv, 16, 16, lds);
            char* dst = KV + (long)t * 4096 + 2048 + lane * 16; const f32x16 a = acc[0][0];
            ST16(dst, ((u32x4){cvtpk(a[0], a[1]), cvtpk(a[2], a[3]), cvtpk(a[4], a[5]), cvtpk(a[6], a[7])}));
            ST16(dst + 1024, ((u32x4){cvtpk(a[8], a[9]), cvtpk(a[10], a[11]), cvtpk(a[12], a[13]), cvtpk(a[14], a[15])}));
        }
    } else {
        const int w = (b - 224) * 8 + wid;
        unsigned sink = 0;
        for (int p = 1; p < 4; ++p) {
            auto rows = [&](const u16* m, int nrows, int off) {
                const char* q = (const char*)m + (long)(lane >> 2) * 2048 + p * 512 + (lane & 3) * 128;
                for (int i = (w + off) & 255; i < nrows / 16; i += 256) asm volatile("global_load_dword %0, %1, off" : "+v"(sink) : "v"(q + (long)i * 32768) : "memory");
            };
            rows(xB, 2048, 0); rows(xA, 2048, 128); rows(dre, 1024, 0); rows(dim, 1024, 64);
            { const char* q = (const char*)wq + (long)p * 524288 + lane * 128; const int i = (w + 128) & 255; if (i < 64) asm volatile("global_load_dword %0, %1, off" : "+v"(sink) : "v"(q + (long)i * 8192) : "memory"); }
        }
        if (w < 64 && wid < CODEPF) {
            unsigned long long c2, c3;
            asm volatile("s_getpc_b64 s[62:63]\n\ts_add_u32 s62, s62, _Z9k2_attn_yPKtPKcPtS0_S0_S0_S3_@rel32@lo+4\n\ts_addc_u32 s63, s63, _Z9k2_attn_yPKtPKcPtS0_S0_S0_S3_@rel32@hi+12" : "={s[62:63]}"(c2) :: "scc");
            asm volatile("s_getpc_b64 s[62:63]\n\ts_add_u32 s62, s62, _Z5k3_woPKtS0_S0_Pt@rel32@lo+4\n\ts_addc_u32 s63, s63, _Z5k3_woPKtS0_S0_Pt@rel32@hi+12" : "={s[62:63]}"(c3) :: "scc");
            const char* q2 = (const char*)c2 + lane * 128; const char* q3 = (const char*)c3 + lane * 128;
            if (wid == 0) { asm volatile("global_load_dword %0, %1, off" : "+v"(sink) : "v"(q2) : "memory"); if (lane < K2_TAIL) asm volatile("global_load_dword %0, %1, off" : "+v"(sink) : "v"(q2 + 8192) : "memory");
                            if (lane < K3_LINES) asm volatile("global_load_dword %0, %1, off" : "+v"(sink) : "v"(q3) : "memory"); }
        }
        { const char* q = (const char*)wo + lane * 128 + (long)w * 8192; asm volatile("global_load_dword %0, %1, off" : "+v"(sink) : "v"(q) : "memory"); }
        asm volatile("s_waitcnt vmcnt(0)" : "+v"(sink) :: "memory");
    }
}
__global__ __launch_bounds__(512, 2) void k2_attn_y(const u16* __restrict__ Q, const char* __restrict__ KV, u16* __restrict__ O, const u16* __restrict__ sre, const u16* __restrict__ sim,
                                                    const u16* __restrict__ abT, u16* __restrict__ P) {
    extern __shared__ __attribute__((aligned(16))) char lds[];
    const int b = blockIdx.x; const int wid = __builtin_amdgcn_readfirstlane(threadIdx.x >> 6);
    const unsigned lds0 = (unsigned)(uintptr_t)lds;
    const int g = b & 7, idx = b >> 3, half = g >> 2;
    const int m0 = ((g & 3) * 4 + (idx & 3)) * 128, n0 = (idx >> 2) * 128;
    if (wid >= 4) {
        fused_produce(wid - 4, KV, (half ? sim : sre) + (long)m0 * 2048, abT + (long)(half * 1024 + n0) * 2048, lds0);
        lds_bar(); lds_bar();
        u16* out = P + (long)half * 2048 * 1024 + (long)m0 * 1024 + n0; const int lane = threadIdx.x & 63;
#pragma unroll
        for (int it = 0; it < 8; ++it) {
            const int row = (it * 4 + wid - 4) * 4 + (lane >> 4), c = lane & 15;
            ST16(out + (long)row * 1024 + c * 8, *(const u32x4*)(lds + row * GYP::ROWB + c * 16));
        }
    } else fused_compute(Q, KV, O, b, wid, lds);
}
__global__ __launch_bounds__(512, 2) void k3_wo(const u16* __restrict__ O, const u16* __restrict__ wo, const u16* __restrict__ P, u16* __restrict__ out) {
    extern __shared__ __attribute__((aligned(16))) char lds[];
    const int t = blockIdx.x, g = t & 7, idx = t >> 3; const int wid = __builtin_amdgcn_readfirstlane(threadIdx.x >> 6);
    const unsigned lds0 = (unsigned)(uintptr_t)lds;
    const int m0 = ((g >> 1) * 4 + (idx & 3)) * 128, n0 = ((g & 1) * 8 + (idx >> 2)) * 64;
    f32x16 acc[2][2];
    if (wid >= 4) {
        u32x4 pa[4], pc[4]; const int tp = threadIdx.x - 256;
#pragma unroll
        for (int i = 0; i < 4; ++i) {
            const int e = (i * 256 + tp) * 8, row = t * 8 + (e >> 10), col = e & 1023;
            const u16* p = P + (long)row * 1024 + col;
            asm volatile("global_load_dwordx4 %0, %1, off" : "=v"(pa[i]) : "v"(p) : "memory");
            asm volatile("global_load_dwordx4 %0, %1, off" : "=v"(pc[i]) : "v"(p + (long)2048 * 1024) : "memory");
        }
        GWO::Prod Pd; GWO::prod_init(Pd, O + (long)m0 * 1024, 1024, wo + n0, 1024, O, wo, 16, lds0);
        GWO::prod_first(Pd, wid - 4);
        asm volatile("" : "+v"(pa[0]), "+v"(pa[1]), "+v"(pa[2]), "+v"(pa[3]), "+v"(pc[0]), "+v"(pc[1]), "+v"(pc[2]), "+v"(pc[3]));
#pragma unroll
        for (int i = 0; i < 4; ++i) {
            const int e = (i * 256 + tp) * 8, row = t * 8 + (e >> 10), col = e & 1023;
            u32x4 r;
#pragma unroll
            for (int j = 0; j < 4; ++j) r[j] = cvtpk(__uint_as_float(pa[i][j] << 16) - __uint_as_float(pc[i][j] << 16), __uint_as_float(pa[i][j] & 0xffff0000u) - __uint_as_float(pc[i][j] & 0xffff0000u));
            ST16(out + (long)row * 2048 + 1024 + col, r);
        }
        GWO::prod_rest(Pd, wid - 4, 16);
    } else GWO::consume(acc, wid, 16, lds0, KL(kb_128x64_nn6k2));
    GWO::epilogue(acc, wid, lds, out + (long)m0 * 2048 + n0, 2048, 1.f);
}

extern "C" void kernel_launch(void* const* d_in, const int* in_sizes, int n_in, void* d_out, int out_size, void* d_ws, size_t ws_size, hipStream_t stream) {
    const u16* xA = (const u16*)d_in[0]; const u16* wq = (const u16*)d_in[1]; const u16* wk = (const u16*)d_in[2]; const u16* wv = (const u16*)d_in[3]; const u16* wo = (const u16*)d_in[4];
    const u16* xB = (const u16*)d_in[5]; const u16* sre = (const u16*)d_in[6]; const u16* sim = (const u16*)d_in[7]; const u16* dre = (const u16*)d_in[8]; const u16* dim = (const u16*)d_in[9];
    u16* out = (u16*)d_out; char* ws = (char*)d_ws;
    u16* Q = (u16*)ws; u16* O = (u16*)(ws + (4 << 20)); u16* abT = (u16*)(ws + (8 << 20)); char* KV = ws + (16 << 20); u16* P = (u16*)(ws + (17 << 20));
    (void)hipFuncSetAttribute((const void*)k1_proj, hipFuncAttributeMaxDynamicSharedMemorySize, L1);
    (void)hipFuncSetAttribute((const void*)k2_attn_y, hipFuncAttributeMaxDynamicSharedMemorySize, L2);
    (void)hipFuncSetAttribute((const void*)k3_wo, hipFuncAttributeMaxDynamicSharedMemorySize, L3);
    k1_proj<<<256, 512, L1, stream>>>(xA, wq, wk, wv, xB, dre, dim, Q, KV, abT, wo);
    k2_attn_y<<<256, 512, L2, stream>>>(Q, KV, O, sre, sim, abT, P);
    k3_wo<<<256, 512, L3, stream>>>(O, wo, P, out);
}
```
